```python
import torch

def _softmax(x, axis=-1):
    return torch.softmax(x, dim=axis)
def _gelu(x):
    return torch.nn.functional.gelu(x, approximate="tanh")
def _silu(x):
    return torch.nn.functional.silu(x)
def _sigmoid(x):
    return torch.sigmoid(x)
def _layernorm(x, axis=-1, eps=1e-5):
    m = x.mean(dim=axis, keepdim=True)
    v = x.var(dim=axis, keepdim=True, unbiased=False)
    return (x - m) / torch.sqrt(v + eps)
def _scatter_add(tgt, idx, src, axis=0):
    return tgt.clone().index_add_(axis, idx, src)
def _segment_sum(src, seg, n):
    out = torch.zeros((n,) + src.shape[1:], dtype=src.dtype)
    return out.index_add_(0, seg, src)
def _pad(x, pw):
    flat = tuple(v for pair in reversed(pw) for v in pair)
    return torch.nn.functional.pad(x, flat)

kernel_name = 'recurrent_scan__sliding_block_cached__glu_gated_swiglu__decode'

def setup_inputs():

    _w = (lambda a: (lambda t: t.to(torch.bfloat16) if t.is_floating_point() else t)(torch.as_tensor(a)))
    def _A_setup():
        B, Sq, D = 4, 1, 512
        Hq, Hkv, Sk, Dh = 32, 8, 4096, 128
        assert Hq % Hkv == 0, (Hq, Hkv)
        return {
            "x": _w((lambda *s: torch.randn(*s, dtype=torch.bfloat16) * 0.05)(B, Sq, D)),
            "k_cache": _w((lambda *s: torch.randn(*s, dtype=torch.bfloat16) * 1.6)(B, Hkv, Sk, Dh)),
            "v_cache": _w((lambda *s: torch.randn(*s, dtype=torch.bfloat16) * 1.6)(B, Hkv, Sk, Dh)),
            "wq": _w((lambda *s: torch.randn(*s, dtype=torch.bfloat16) * 1.6)(D, Hq * Dh) * (D ** -0.5)),
            "wo": _w((lambda *s: torch.randn(*s, dtype=torch.bfloat16) * 1.6)(Hq * Dh, D) * ((Hq * Dh) ** -0.5)),
        }
    _A_in = _A_setup()

    def _B_setup():
        B, S, D, F = 4, 1, 512, 1792
        return {
            "x": _w((lambda *s: torch.randn(*s, dtype=torch.bfloat16) * 0.05)(B, S, D)),
            "w1": _w((lambda *s: torch.randn(*s, dtype=torch.bfloat16) * 0.2)(D, F) * (2.0 / D) ** 0.5),
            "w3": _w((lambda *s: torch.randn(*s, dtype=torch.bfloat16) * 0.2)(D, F) * (2.0 / D) ** 0.5),
            "w2": _w((lambda *s: torch.randn(*s, dtype=torch.bfloat16) * 0.2)(F, D) * (2.0 / F) ** 0.5),
        }
    _B_in = _B_setup()

    return {
        "A_x": _A_in["x"],
        "A_k_cache": _A_in["k_cache"],
        "A_v_cache": _A_in["v_cache"],
        "A_wq": _A_in["wq"],
        "A_wo": _A_in["wo"],
        "B_x": _B_in["x"],
        "B_w1": _B_in["w1"],
        "B_w3": _B_in["w3"],
        "B_w2": _B_in["w2"],
    }

def reference(A_x, A_k_cache, A_v_cache, A_wq, A_wo, B_x, B_w1, B_w3, B_w2):
    def _A_ref(x, k_cache, v_cache, wq, wo):
        B, Sq, D = x.shape
        Hq, Dh = 32, 128
        Hkv, Sk = k_cache.shape[1], k_cache.shape[2]
        W = min(4096, max(256, Sk // 4))
        rep = Hq // Hkv
        q = (lambda x, *d: x.permute(*d))((x @ wq).reshape(B, Sq, Hq, Dh), 0, 2, 1, 3)
        k = (lambda x, n, axis: torch.repeat_interleave(x, n, dim=axis))(k_cache[:, :, Sk - W:, :], rep, axis=1)
        v = (lambda x, n, axis: torch.repeat_interleave(x, n, dim=axis))(v_cache[:, :, Sk - W:, :], rep, axis=1)
        scores = torch.einsum("bhqd,bhkd->bhqk", q, k) * (Dh ** -0.5)
        p = _softmax(scores, axis=-1)
        o = torch.einsum("bhqk,bhkd->bhqd", p, v)
        return (lambda x, *d: x.permute(*d))(o, 0, 2, 1, 3).reshape(B, Sq, Hq * Dh) @ wo

    def _B_ref(x, w1, w3, w2):
        gate = torch.einsum("bsd,df->bsf", x, w1)
        up = torch.einsum("bsd,df->bsf", x, w3)
        h = _silu(gate) * up
        out = torch.einsum("bsf,fd->bsd", h, w2)
        return out

    x = A_x
    xn = _layernorm(x, axis=-1)
    s = _A_ref(xn, A_k_cache, A_v_cache, A_wq, A_wo)
    g = _silu(_B_ref(xn, B_w1, B_w3, B_w2))
    return x + s * g
```

```cpp
#include <hip/hip_runtime.h>
#include <hip/hip_bf16.h>
typedef unsigned short u16;
__device__ __forceinline__ float bf2f(u16 v) { return __uint_as_float(((unsigned)v) << 16); }
__device__ __forceinline__ u16 f2bf(float f) { unsigned u = __float_as_uint(f); u += 0x7FFFu + ((u >> 16) & 1u); return (u16)(u >> 16); }
__device__ __forceinline__ float rbf(float f) { return bf2f(f2bf(f)); }
constexpr int B = 4, D = 512, HQ = 32, HKV = 8, SK = 4096, DH = 128, W = 1024, F = 1792;
__device__ void ln(const u16* x, float* xn  , float* red) {
  int tid = threadIdx.x;
  for (int b = 0; b < B; ++b) {
    float v0 = bf2f(x[b * D + tid]), v1 = bf2f(x[b * D + tid + 256]);
    red[tid] = v0 + v1; __syncthreads();
    for (int s = 128; s > 0; s >>= 1) { if (tid < s) red[tid] += red[tid + s]; __syncthreads(); }
    float m = red[0] / D; __syncthreads();
    red[tid] = (v0 - m) * (v0 - m) + (v1 - m) * (v1 - m); __syncthreads();
    for (int s = 128; s > 0; s >>= 1) { if (tid < s) red[tid] += red[tid + s]; __syncthreads(); }
    float var = red[0] / D; __syncthreads();
    float r = 1.0f / sqrtf(var + 1e-5f);
    xn[b * D + tid] = rbf((v0 - m) * r); xn[b * D + tid + 256] = rbf((v1 - m) * r);
  }
  __syncthreads();
}
__global__ void k1(const u16* x, const u16* wq, const u16* w1, const u16* w3, float* q, float* h) {
  __shared__ float xn[B * D]; __shared__ float red[256];
  ln(x, xn, red);
  int col = blockIdx.x * 256 + threadIdx.x;
  if (col < 4096) {
    float a[B] = {0, 0, 0, 0};
    for (int d = 0; d < D; ++d) { float w = bf2f(wq[(size_t)d * 4096 + col]); for (int b = 0; b < B; ++b) a[b] += xn[b * D + d] * w; }
    for (int b = 0; b < B; ++b) q[b * 4096 + col] = rbf(a[b]);
  } else {
    int c = col - 4096; if (c < F) {
      float g[B] = {0, 0, 0, 0}, u[B] = {0, 0, 0, 0};
      for (int d = 0; d < D; ++d) { float a = bf2f(w1[(size_t)d * F + c]), bb = bf2f(w3[(size_t)d * F + c]); for (int b = 0; b < B; ++b) { g[b] += xn[b * D + d] * a; u[b] += xn[b * D + d] * bb; } }
      for (int b = 0; b < B; ++b) h[b * F + c] = rbf(g[b] / (1.f + __expf(-g[b])) * u[b]);
    }
  }
}
__global__ void k2(const float* q, const u16* kc, const u16* vc, float* o) {
  __shared__ float sq[DH]; __shared__ float p[W]; __shared__ float red[256];
  int b = blockIdx.x / HQ, h = blockIdx.x % HQ, kv = h / 4, tid = threadIdx.x;
  if (tid < DH) sq[tid] = q[b * 4096 + h * DH + tid];
  __syncthreads();
  const u16* K = kc + ((size_t)(b * HKV + kv) * SK + (SK - W)) * DH;
  const u16* V = vc + ((size_t)(b * HKV + kv) * SK + (SK - W)) * DH;
  float mx = -INFINITY;
  for (int k = tid; k < W; k += 256) { float s = 0; for (int d = 0; d < DH; ++d) s += sq[d] * bf2f(K[(size_t)k * DH + d]); s *= 0.08838834764831845f; p[k] = s; mx = fmaxf(mx, s); }
  red[tid] = mx; __syncthreads();
  for (int s = 128; s > 0; s >>= 1) { if (tid < s) red[tid] = fmaxf(red[tid], red[tid + s]); __syncthreads(); }
  mx = red[0]; __syncthreads();
  float l = 0; for (int k = tid; k < W; k += 256) { float e = __expf(p[k] - mx); p[k] = e; l += e; }
  red[tid] = l; __syncthreads();
  for (int s = 128; s > 0; s >>= 1) { if (tid < s) red[tid] += red[tid + s]; __syncthreads(); }
  l = red[0];
  if (tid < DH) { float a = 0; for (int k = 0; k < W; ++k) a += p[k] * bf2f(V[(size_t)k * DH + tid]); o[b * 4096 + h * DH + tid] = rbf(a / l); }
}
__global__ void k3(const u16* x, const float* o, const float* h, const u16* wo, const u16* w2, u16* out) {
  __shared__ float rs[4][B][64], rf[4][B][64];
  int c = blockIdx.x * 64 + (threadIdx.x & 63), g = threadIdx.x >> 6;
  float s[B] = {0, 0, 0, 0}, f[B] = {0, 0, 0, 0};
  for (int k = g; k < 4096; k += 4) { float w = bf2f(wo[(size_t)k * D + c]); for (int b = 0; b < B; ++b) s[b] += o[b * 4096 + k] * w; }
  for (int k = g; k < F; k += 4) { float w = bf2f(w2[(size_t)k * D + c]); for (int b = 0; b < B; ++b) f[b] += h[b * F + k] * w; }
  for (int b = 0; b < B; ++b) { rs[g][b][threadIdx.x & 63] = s[b]; rf[g][b][threadIdx.x & 63] = f[b]; }
  __syncthreads();
  if (g == 0) for (int b = 0; b < B; ++b) {
    int l = threadIdx.x & 63;
    float ss = rs[0][b][l] + rs[1][b][l] + rs[2][b][l] + rs[3][b][l];
    float ff = rf[0][b][l] + rf[1][b][l] + rf[2][b][l] + rf[3][b][l];
    float gg = ff / (1.f + __expf(-ff));
    out[b * D + c] = f2bf(bf2f(x[b * D + c]) + ss * gg);
  }
}
extern "C" void kernel_launch(void* const* d_in, const int* in_sizes, int n_in,
                              void* d_out, int out_size, void* d_ws, size_t ws_size,
                              hipStream_t stream) {
  const u16* x = (const u16*)d_in[0]; const u16* kc = (const u16*)d_in[1]; const u16* vc = (const u16*)d_in[2];
  const u16* wq = (const u16*)d_in[3]; const u16* wo = (const u16*)d_in[4];
  const u16* w1 = (const u16*)d_in[6]; const u16* w3 = (const u16*)d_in[7]; const u16* w2 = (const u16*)d_in[8];
  float* q = (float*)d_ws; float* h = q + B * 4096; float* o = h + B * F;
  k1<<<(4096 + F) / 256, 256, 0, stream>>>(x, wq, w1, w3, q, h);
  k2<<<B * HQ, 256, 0, stream>>>(q, kc, vc, o);
  k3<<<D / 64, 256, 0, stream>>>(x, o, h, wo, w2, (u16*)d_out);
}
```

```cpp
#include <hip/hip_runtime.h>
#include <stdint.h>

namespace {
typedef unsigned short u16;
typedef unsigned u32;
typedef unsigned long long u64;
typedef u32 u32x4 __attribute__((ext_vector_type(4)));
typedef u32 u32x2 __attribute__((ext_vector_type(2)));
typedef float f32x4 __attribute__((ext_vector_type(4)));
typedef float f32x2 __attribute__((ext_vector_type(2)));
typedef short s16x4 __attribute__((ext_vector_type(4)));
typedef __bf16 bf16x2 __attribute__((ext_vector_type(2)));
typedef __bf16 bf16x8 __attribute__((ext_vector_type(8)));
#define LAS __attribute__((address_space(3)))
#define GAS __attribute__((address_space(1)))

constexpr u32 WS_EPOCH = 0;
constexpr u32 WS_Q = 4096;
constexpr u32 WS_G = WS_Q + 65536;
constexpr u32 WS_S = WS_G + 57344;
constexpr u32 WS_O = WS_S + 16384;
constexpr u32 WS_H = WS_O + 524288;
constexpr u32 WS_END = WS_H + 256 * 96 * 64;
constexpr u32 WS_DBG = (WS_END + 4095) & ~4095u;

constexpr u32 L_WQ = 0, L_WG = 16384, L_K = 32768, L_V = 65536, L_WO = 98304, L_W2 = 114688;
constexpr u32 L_XN = 122880, XN_S = 1088;
constexpr u32 L_QP = L_XN + 4 * XN_S;
constexpr u32 L_GP = L_QP + 2048;
constexpr u32 L_QI = L_GP + 2048, QI_S = 320;
constexpr u32 L_PI = L_QI + 4 * QI_S;
constexpr u32 L_MX = L_PI + 4 * QI_S;
constexpr u32 L_OA = L_MX + 128, OA_S = 192;
constexpr u32 L_HA = L_OA + 4 * OA_S;
constexpr u32 L_TAG = L_HA + 4 * OA_S;
constexpr u32 L_END = L_TAG + 16;

constexpr float SM_C = 0.08838834764831845f * 1.4426950408889634f;
constexpr float LOG2E = 1.4426950408889634f;
constexpr int SPIN_CAP = 1 << 16;
constexpr int AUX_SC1 = 16;

#define KV_NT ""
#define W_NT ""

__device__ __forceinline__ void dma_kv(const void* sbase, u32 voff, u32 lds) {
  asm volatile("s_mov_b32 m0, %2\n\ts_nop 4\n\tglobal_load_lds_dwordx4 %0, %1" KV_NT ::"v"(voff), "s"(sbase), "s"(lds) : "memory");
}
__device__ __forceinline__ void dma_w(const void* sbase, u32 voff, u32 lds) {
  asm volatile("s_mov_b32 m0, %2\n\ts_nop 4\n\tglobal_load_lds_dwordx4 %0, %1" W_NT ::"v"(voff), "s"(sbase), "s"(lds) : "memory");
}
#define VMWAIT(n) asm volatile("s_waitcnt vmcnt(" #n ")" ::: "memory")
#define BAR() asm volatile("s_waitcnt lgkmcnt(0)\n\ts_barrier" ::: "memory")

__device__ __forceinline__ u32 pk(float lo, float hi) {
  f32x2 v = {lo, hi};
  return __builtin_bit_cast(u32, __builtin_convertvector(v, bf16x2));
}
__device__ __forceinline__ float bflo(u32 v) { return __uint_as_float(v << 16); }
__device__ __forceinline__ float bfhi(u32 v) { return __uint_as_float(v & 0xffff0000u); }
__device__ __forceinline__ f32x4 mfma(u32x4 a, u32x4 b, f32x4 c) {
  return __builtin_amdgcn_mfma_f32_16x16x32_bf16(__builtin_bit_cast(bf16x8, a), __builtin_bit_cast(bf16x8, b), c, 0, 0, 0);
}
__device__ __forceinline__ u32x4 lds128(LAS unsigned char* L, u32 off) { return *(LAS u32x4*)(L + off); }
__device__ __forceinline__ u32x4 tr2(LAS unsigned char* L, u32 off0, u32 off1) {
  u32x2 a = __builtin_bit_cast(u32x2, __builtin_amdgcn_ds_read_tr16_b64_v4i16((LAS s16x4*)(L + off0)));
  u32x2 b = __builtin_bit_cast(u32x2, __builtin_amdgcn_ds_read_tr16_b64_v4i16((LAS s16x4*)(L + off1)));
  return u32x4{a.x, a.y, b.x, b.y};
}
template <int CTRL> __device__ __forceinline__ float dpp(float x) {
  return __builtin_bit_cast(float, __builtin_amdgcn_mov_dpp(__builtin_bit_cast(int, x), CTRL, 0xf, 0xf, true));
}
__device__ __forceinline__ float xrow_sum(float x) {
  auto s = __builtin_amdgcn_permlane16_swap(__float_as_uint(x), __float_as_uint(x), false, false);
  x = __uint_as_float(s[0]) + __uint_as_float(s[1]);
  auto t = __builtin_amdgcn_permlane32_swap(__float_as_uint(x), __float_as_uint(x), false, false);
  return __uint_as_float(t[0]) + __uint_as_float(t[1]);
}
__device__ __forceinline__ float xrow_max(float x) {
  auto s = __builtin_amdgcn_permlane16_swap(__float_as_uint(x), __float_as_uint(x), false, false);
  x = fmaxf(__uint_as_float(s[0]), __uint_as_float(s[1]));
  auto t = __builtin_amdgcn_permlane32_swap(__float_as_uint(x), __float_as_uint(x), false, false);
  return fmaxf(__uint_as_float(t[0]), __uint_as_float(t[1]));
}
__device__ __forceinline__ float wave_sum(float x) {
  x += dpp<0xB1>(x);
  x += dpp<0x4E>(x);
  x += dpp<0x141>(x);
  x += dpp<0x140>(x);
  return xrow_sum(x);
}
__device__ __forceinline__ u32x4 ldmb(__amdgpu_buffer_rsrc_t rs, u32 off) { return __builtin_amdgcn_raw_buffer_load_b128(rs, (int)off, 0, AUX_SC1); }
__device__ __forceinline__ void stmb(__amdgpu_buffer_rsrc_t rs, u32 off, u32x4 v) { __builtin_amdgcn_raw_buffer_store_b128(v, rs, (int)off, 0, AUX_SC1); }
__device__ __forceinline__ bool tagok(u32x4 v, u32 tA, u32 tB) { return v.y == tA && v.w == tB; }
__device__ __forceinline__ bool allok(bool ok) { return __builtin_amdgcn_ballot_w64(!ok) == 0ull; }

#define STAMP(k)
#define STAMP_DECL
#define STAMP_FLUSH()
}

__global__ void __launch_bounds__(768) fused_decode_kernel(const u16* __restrict__ xp, const u16* __restrict__ kc, const u16* __restrict__ vc,
                                                           const u16* __restrict__ wq, const u16* __restrict__ wo, const u16* __restrict__ w1,
                                                           const u16* __restrict__ w3, const u16* __restrict__ w2, u16* __restrict__ out,
                                                           void* __restrict__ ws) {
  __shared__ __attribute__((aligned(16))) unsigned char smem[L_END];
  LAS unsigned char* L = (LAS unsigned char*)smem;
  const u32 lb = (u32)(uintptr_t)L;
  const int tid = threadIdx.x, lane = tid & 63;
  const int w = __builtin_amdgcn_readfirstlane(tid >> 6);
  const int c = blockIdx.x, x = c & 7, j = c >> 3;
  const bool ffn = j < 28;
  const int n16 = lane & 15, g = lane >> 4;
  const __amdgpu_buffer_rsrc_t rs = __builtin_amdgcn_make_buffer_rsrc(ws, 0, (int)WS_END, 0x00020000);
  STAMP_DECL;
  STAMP(0);

  if (w < 8) {
    const int b = j >> 3, s = j & 7;
    const size_t kvb = ((size_t)((b * 8 + x) * 4096 + 3072 + 128 * s + 16 * w)) * 256;
    const char* kb = (const char*)kc + kvb;
    const char* vb = (const char*)vc + kvb;
    const int lg = 7 * x + (j >> 2), mtx = lg & 1, G1 = lg >> 1;
    {
      const char* sb = (const char*)wq + (size_t)(64 * w) * 8192 + (512 * x + 16 * j) * 2;
      const u32 vo = (u32)(lane >> 1) * 8192 + (lane & 1) * 16;
      dma_w(sb, vo, lb + L_WQ + (64 * w) * 32);
      dma_w(sb + 32 * 8192, vo, lb + L_WQ + (64 * w + 32) * 32);
    }
    if (ffn) {
      const char* sb = (const char*)(mtx ? w3 : w1) + (size_t)(64 * w) * 3584 + (64 * G1 + 16 * (j & 3)) * 2;
      const u32 vo = (u32)(lane >> 1) * 3584 + (lane & 1) * 16;
      dma_w(sb, vo, lb + L_WG + (64 * w) * 32);
      dma_w(sb + 32 * 3584, vo, lb + L_WG + (64 * w + 32) * 32);
    }
#pragma unroll
    for (int i = 0; i < 4; ++i) {
      const u32 r = 4 * i + g;
      dma_kv(kb, r * 256 + ((n16 ^ r) * 16), lb + L_K + (16 * w + 4 * i) * 256);
    }
#pragma unroll
    for (int i = 0; i < 4; ++i) {
      const u32 r = 4 * i + g;
      dma_kv(vb, r * 256 + ((n16 ^ ((g << 2) | i)) * 16), lb + L_V + (16 * w + 4 * i) * 256);
    }
    {
      const int dt = j >> 2, cg4 = j & 3;
      const char* sb = (const char*)wo + (size_t)((4 * x + (w >> 1)) * 128 + 16 * dt + 8 * (w & 1)) * 1024 + cg4 * 256;
#pragma unroll
      for (int i = 0; i < 2; ++i)
        dma_w(sb, (u32)(4 * i + g) * 1024 + ((n16 ^ ((g << 2) | ((2 * w + i) & 3))) * 16), lb + L_WO + (8 * w + 4 * i) * 256);
    }
    if (ffn) {
      const char* sb = (const char*)w2 + (size_t)(64 * j + 8 * w) * 1024 + x * 128;
      dma_w(sb, (u32)(lane >> 3) * 1024 + ((((lane & 7) >> 1) ^ (g & 3)) * 32) + (lane & 1) * 16, lb + L_W2 + 8 * w * 128);
    }
    STAMP(1);
    BAR();
    STAMP(2);
    const u32 tA = *(LAS u32*)(L + L_TAG), tB = *(LAS u32*)(L + L_TAG + 4);
    {
      const u32 xo = L_XN + (n16 & 3) * XN_S + g * 16 + 2 * w * 64;
      const u32x4 xa = lds128(L, xo), xb = lds128(L, xo + 64);
      if (ffn) VMWAIT(13); else VMWAIT(10);
      f32x4 acc = {0.f, 0.f, 0.f, 0.f};
      const u32 o = L_WQ + (64 * w) * 32 + lane * 8;
      acc = mfma(tr2(L, o, o + 512), xa, acc);
      acc = mfma(tr2(L, o + 1024, o + 1536), xb, acc);
      if (n16 < 4) *(LAS f32x4*)(L + L_QP + w * 256 + n16 * 64 + g * 16) = acc;
      if (ffn) {
        VMWAIT(11);
        f32x4 a2 = {0.f, 0.f, 0.f, 0.f};
        const u32 o2 = L_WG + (64 * w) * 32 + lane * 8;
        a2 = mfma(tr2(L, o2, o2 + 512), xa, a2);
        a2 = mfma(tr2(L, o2 + 1024, o2 + 1536), xb, a2);
        if (n16 < 4) *(LAS f32x4*)(L + L_GP + w * 256 + n16 * 64 + g * 16) = a2;
      }
    }
    STAMP(3);
    BAR();
    if (ffn) VMWAIT(7); else VMWAIT(6);
    STAMP(4);
    u32x4 kf[4];
#pragma unroll
    for (int st = 0; st < 4; ++st) kf[st] = lds128(L, L_K + (16 * w + n16) * 256 + (((4 * st + g) ^ n16) * 16));
    BAR();
    STAMP(5);
    f32x4 sc = {0.f, 0.f, 0.f, 0.f};
#pragma unroll
    for (int st = 0; st < 4; ++st) sc = mfma(kf[st], lds128(L, L_QI + (n16 & 3) * QI_S + st * 64 + g * 16), sc);
    float mx = xrow_max(fmaxf(fmaxf(sc.x, sc.y), fmaxf(sc.z, sc.w)));
    if (lane < 4) *(LAS float*)(L + L_MX + lane * 32 + w * 4) = mx;
    BAR();
    float Mc;
    {
      const f32x4 m0 = *(LAS f32x4*)(L + L_MX + (n16 & 3) * 32), m1 = *(LAS f32x4*)(L + L_MX + (n16 & 3) * 32 + 16);
      Mc = fmaxf(fmaxf(fmaxf(m0.x, m0.y), fmaxf(m0.z, m0.w)), fmaxf(fmaxf(m1.x, m1.y), fmaxf(m1.z, m1.w))) * SM_C;
      const float p0 = __builtin_amdgcn_exp2f(fmaf(sc.x, SM_C, -Mc)), p1 = __builtin_amdgcn_exp2f(fmaf(sc.y, SM_C, -Mc));
      const float p2 = __builtin_amdgcn_exp2f(fmaf(sc.z, SM_C, -Mc)), p3 = __builtin_amdgcn_exp2f(fmaf(sc.w, SM_C, -Mc));
      if (n16 < 4) *(LAS u32x2*)(L + L_PI + n16 * QI_S + (16 * w + 4 * g) * 2) = u32x2{pk(p0, p1), pk(p2, p3)};
    }
    if (ffn) VMWAIT(3); else VMWAIT(2);
    STAMP(6);
    BAR();
    {
      const int q4 = n16 >> 2, p4 = lane & 3;
      f32x4 o = {0.f, 0.f, 0.f, 0.f}, l = {0.f, 0.f, 0.f, 0.f};
      const u32x4 ones = {0x3F803F80u, 0x3F803F80u, 0x3F803F80u, 0x3F803F80u};
#pragma unroll
      for (int st = 0; st < 4; ++st) {
        const u32x4 pf = lds128(L, L_PI + (n16 & 3) * QI_S + st * 64 + g * 16);
        const u32 r0 = 32 * st + 8 * g + q4, r1 = r0 + 4;
        const u32 ch = 2 * w + (p4 >> 1);
        const u32 a0 = L_V + r0 * 256 + ((ch ^ ((q4 << 2) | ((2 * g) & 3))) * 16) + 8 * (p4 & 1);
        const u32 a1 = L_V + r1 * 256 + ((ch ^ ((q4 << 2) | ((2 * g + 1) & 3))) * 16) + 8 * (p4 & 1);
        o = mfma(pf, tr2(L, a0, a1), o);
        l = mfma(pf, ones, l);
      }
      if (lane < 16) stmb(rs, WS_O + (((x * 8 + w) * 32 + j) * 16 + lane) * 16, u32x4{pk(o.x, o.y), tA, pk(o.z, o.w), tB});
      if (w == 0 && lane < 4) {
        const float lv = lane == 0 ? l.x : lane == 1 ? l.y : lane == 2 ? l.z : l.w;
        stmb(rs, WS_S + ((x * 32 + j) * 4 + lane) * 16, u32x4{__float_as_uint(Mc), tA, __float_as_uint(lv), tB});
      }
    }
    STAMP(7);
    if (w == 0) VMWAIT(2); else VMWAIT(1);
    STAMP(8);
    BAR();
    STAMP(9);
    {
      const int q4 = n16 >> 2, p4 = lane & 3;
      f32x4 a = {0.f, 0.f, 0.f, 0.f};
#pragma unroll
      for (int st = 0; st < 2; ++st) {
        const u32x4 of = lds128(L, L_OA + (n16 & 3) * OA_S + st * 64 + g * 16);
        const u32 r0 = 32 * st + 8 * g + q4, r1 = r0 + 4;
        const u32 ch = 2 * w + (p4 >> 1);
        const u32 a0 = L_WO + r0 * 256 + ((ch ^ ((q4 << 2) | ((2 * g) & 3))) * 16) + 8 * (p4 & 1);
        const u32 a1 = L_WO + r1 * 256 + ((ch ^ ((q4 << 2) | ((2 * g + 1) & 3))) * 16) + 8 * (p4 & 1);
        a = mfma(of, tr2(L, a0, a1), a);
      }
      if (lane < 16) {
        const int cq = 32 * (j & 3) + 4 * w + (lane >> 2), slot = x * 8 + (j >> 2);
        const u32 o0 = WS_H + (((cq * 2) * 96 + slot) * 4 + (lane & 3)) * 16;
        stmb(rs, o0, u32x4{__float_as_uint(a.x), tA, __float_as_uint(a.y), tB});
        stmb(rs, o0 + 96 * 64, u32x4{__float_as_uint(a.z), tA, __float_as_uint(a.w), tB});
      }
      if (ffn && w < 4) {
        f32x4 f = {0.f, 0.f, 0.f, 0.f};
#pragma unroll
        for (int st = 0; st < 2; ++st) {
          const u32x4 hf = lds128(L, L_HA + (n16 & 3) * OA_S + st * 64 + g * 16);
          const u32 r0 = 32 * st + 4 * g + q4, r1 = r0 + 16;
          const u32 ph = (u32)(w ^ ((2 * g + (q4 >> 1)) & 3));
          f = mfma(hf, tr2(L, L_W2 + r0 * 128 + ph * 32 + 8 * p4, L_W2 + r1 * 128 + ph * 32 + 8 * p4), f);
        }
        if (lane < 16) {
          const int cq = 16 * x + 4 * w + (lane >> 2), slot = 64 + j;
          const u32 o0 = WS_H + (((cq * 2) * 96 + slot) * 4 + (lane & 3)) * 16;
          stmb(rs, o0, u32x4{__float_as_uint(f.x), tA, __float_as_uint(f.y), tB});
          stmb(rs, o0 + 96 * 64, u32x4{__float_as_uint(f.z), tA, __float_as_uint(f.w), tB});
        }
      }
    }
    STAMP(10);
    STAMP_FLUSH();
  } else {
    const int cb = w - 8;
    u64 E = 0;
    if (cb == 0) E = __hip_atomic_load((GAS u64*)ws, __ATOMIC_RELAXED, __HIP_MEMORY_SCOPE_AGENT);
    const u32x4 xv = *(const GAS u32x4*)(xp + cb * 512 + lane * 8);
    const int cq = c >> 1, bp = c & 1;
    u32 xr0 = 0, xr1 = 0;
    if (cb == 0) {
      xr0 = ((const GAS u16*)xp)[(2 * bp) * 512 + 4 * cq + (lane & 3)];
      xr1 = ((const GAS u16*)xp)[(2 * bp + 1) * 512 + 4 * cq + (lane & 3)];
    }
    {
      float v[8] = {bflo(xv.x), bfhi(xv.x), bflo(xv.y), bfhi(xv.y), bflo(xv.z), bfhi(xv.z), bflo(xv.w), bfhi(xv.w)};
      float sm = ((v[0] + v[1]) + (v[2] + v[3])) + ((v[4] + v[5]) + (v[6] + v[7]));
      const float mean = wave_sum(sm) * (1.0f / 512.0f);
      float sq = 0.f;
#pragma unroll
      for (int e = 0; e < 8; ++e) { v[e] -= mean; sq = fmaf(v[e], v[e], sq); }
      const float rstd = 1.0f / sqrtf(wave_sum(sq) * (1.0f / 512.0f) + 1e-5f);
      const u32 o = L_XN + cb * XN_S + (lane >> 2) * 64 + (lane & 1) * 32 + ((lane & 3) >> 1) * 8;
      *(LAS u32x2*)(L + o) = u32x2{pk(v[0] * rstd, v[1] * rstd), pk(v[2] * rstd, v[3] * rstd)};
      *(LAS u32x2*)(L + o + 16) = u32x2{pk(v[4] * rstd, v[5] * rstd), pk(v[6] * rstd, v[7] * rstd)};
    }
    u32 tA, tB;
    u64 T = 0;
    if (cb == 0) {
      T = E + 1;
      tA = (u32)T;
      tB = (u32)(E >> 32) ^ ((tA << 1) | 1u);
      if (lane == 0) *(LAS u32x2*)(L + L_TAG) = u32x2{tA, tB};
    }
    STAMP(1);
    BAR();
    STAMP(2);
    tA = *(LAS u32*)(L + L_TAG);
    tB = *(LAS u32*)(L + L_TAG + 4);
    BAR();
    STAMP(3);
    if (cb == 0) {
      f32x4 a = *(LAS f32x4*)(L + L_QP + lane * 16), a2 = *(LAS f32x4*)(L + L_QP + 1024 + lane * 16);
      a += a2;
      a.x = xrow_sum(a.x); a.y = xrow_sum(a.y); a.z = xrow_sum(a.z); a.w = xrow_sum(a.w);
      if (lane < 16) stmb(rs, WS_Q + ((x * 4 + (lane >> 2)) * 128 + 4 * j + (lane & 3)) * 16, u32x4{pk(a.x, a.y), tA, pk(a.z, a.w), tB});
      STAMP(4);
      const u32 qo = WS_Q + ((x * 4 + (j >> 3)) * 128 + lane) * 16;
      u32x4 g0, g1;
      for (int it = 0; it < SPIN_CAP; ++it) {
        asm volatile("" ::: "memory");
        g0 = ldmb(rs, qo);
        g1 = ldmb(rs, qo + 1024);
        if (allok(tagok(g0, tA, tB) && tagok(g1, tA, tB))) break;
      }
      *(LAS u32x2*)(L + L_QI + (lane >> 5) * QI_S + (lane & 31) * 8) = u32x2{g0.x, g0.z};
      *(LAS u32x2*)(L + L_QI + (2 + (lane >> 5)) * QI_S + (lane & 31) * 8) = u32x2{g1.x, g1.z};
      STAMP(5);
    } else if (cb == 1 && ffn) {
      f32x4 a = *(LAS f32x4*)(L + L_GP + lane * 16), a2 = *(LAS f32x4*)(L + L_GP + 1024 + lane * 16);
      a += a2;
      a.x = xrow_sum(a.x); a.y = xrow_sum(a.y); a.z = xrow_sum(a.z); a.w = xrow_sum(a.w);
      const int lg = 7 * x + (j >> 2);
      if (lane < 16) stmb(rs, WS_G + ((lg * 4 + (lane >> 2)) * 16 + 4 * (j & 3) + (lane & 3)) * 16, u32x4{pk(a.x, a.y), tA, pk(a.z, a.w), tB});
    }
    BAR();
    BAR();
    BAR();
    STAMP(6);
    {
      const u32 so = WS_S + ((x * 32 + 8 * cb + 2 * g) * 4 + (n16 & 3)) * 16;
      const u32 oo = WS_O + (((x * 8 + (j >> 2)) * 32 + 8 * cb + 2 * g) * 16 + n16) * 16;
      u32x4 sa, sb, da, db;
      if (cb == 1 && ffn) {
        const u32 go = WS_G + ((2 * j) * 64 + lane) * 16;
        u32x4 gg, uu;
        for (int it = 0; it < SPIN_CAP; ++it) {
          asm volatile("" ::: "memory");
          gg = ldmb(rs, go);
          uu = ldmb(rs, go + 1024);
          if (allok(tagok(gg, tA, tB) && tagok(uu, tA, tB))) break;
        }
        const float g0 = bflo(gg.x), g1 = bfhi(gg.x), g2 = bflo(gg.z), g3 = bfhi(gg.z);
        const float h0 = g0 * __builtin_amdgcn_rcpf(1.f + __builtin_amdgcn_exp2f(-g0 * LOG2E)) * bflo(uu.x);
        const float h1 = g1 * __builtin_amdgcn_rcpf(1.f + __builtin_amdgcn_exp2f(-g1 * LOG2E)) * bfhi(uu.x);
        const float h2 = g2 * __builtin_amdgcn_rcpf(1.f + __builtin_amdgcn_exp2f(-g2 * LOG2E)) * bflo(uu.z);
        const float h3 = g3 * __builtin_amdgcn_rcpf(1.f + __builtin_amdgcn_exp2f(-g3 * LOG2E)) * bfhi(uu.z);
        *(LAS u32x2*)(L + L_HA + g * OA_S + (n16 >> 3) * 64 + (n16 & 3) * 16 + ((n16 & 7) >> 2) * 8) = u32x2{pk(h0, h1), pk(h2, h3)};
      }
      for (int it = 0; it < SPIN_CAP; ++it) {
        asm volatile("" ::: "memory");
        sa = ldmb(rs, so); sb = ldmb(rs, so + 64);
        da = ldmb(rs, oo); db = ldmb(rs, oo + 256);
        if (allok(tagok(sa, tA, tB) && tagok(sb, tA, tB) && tagok(da, tA, tB) && tagok(db, tA, tB))) break;
      }
      STAMP(7);
      const float Ma = __uint_as_float(sa.x), La = __uint_as_float(sa.z), Mb = __uint_as_float(sb.x), Lb = __uint_as_float(sb.z);
      const float Mx = xrow_max(fmaxf(Ma, Mb));
      const float ea = __builtin_amdgcn_exp2f(Ma - Mx), eb = __builtin_amdgcn_exp2f(Mb - Mx);
      const float r = 1.0f / xrow_sum(fmaf(ea, La, eb * Lb));
      const u32 wp = pk(ea * r, eb * r);
      const u32 sh = (n16 & 1) * 16;
      const u32 A_ = (wp & 0xffffu) << sh, B_ = (wp >> 16) << sh;
      const bool lo = (n16 & 2) == 0;
      const u32x4 wf = {lo ? A_ : 0u, lo ? 0u : A_, lo ? B_ : 0u, lo ? 0u : B_};
      f32x4 d = {0.f, 0.f, 0.f, 0.f};
      d = mfma(u32x4{da.x, da.z, db.x, db.z}, wf, d);
      if (n16 < 4) *(LAS u32x2*)(L + L_OA + cb * OA_S + (16 * n16 + 4 * g) * 2) = u32x2{pk(d.x, d.y), pk(d.z, d.w)};
    }
    STAMP(8);
    BAR();
    if (cb == 0) {
      const u32 ho = WS_H + (c * 96 * 4 + lane) * 16;
      u32x4 h[6];
      const bool v5 = lane < 48;
      for (int it = 0; it < SPIN_CAP; ++it) {
        asm volatile("" ::: "memory");
        bool ok = true;
#pragma unroll
        for (int i = 0; i < 5; ++i) h[i] = ldmb(rs, ho + i * 1024);
        h[5] = ldmb(rs, ho + (v5 ? 5 : 4) * 1024);
#pragma unroll
        for (int i = 0; i < 6; ++i) ok = ok && tagok(h[i], tA, tB);
        if (allok(ok)) break;
      }
      STAMP(9);
      float s0 = 0.f, s1 = 0.f;
#pragma unroll
      for (int i = 0; i < 4; ++i) { s0 += __uint_as_float(h[i].x); s1 += __uint_as_float(h[i].z); }
      float f0 = __uint_as_float(h[4].x) + (v5 ? __uint_as_float(h[5].x) : 0.f);
      float f1 = __uint_as_float(h[4].z) + (v5 ? __uint_as_float(h[5].z) : 0.f);
      s0 += dpp<0x124>(s0); s1 += dpp<0x124>(s1); f0 += dpp<0x124>(f0); f1 += dpp<0x124>(f1);
      s0 += dpp<0x128>(s0); s1 += dpp<0x128>(s1); f0 += dpp<0x128>(f0); f1 += dpp<0x128>(f1);
      s0 = xrow_sum(s0); s1 = xrow_sum(s1); f0 = xrow_sum(f0); f1 = xrow_sum(f1);
      const float y0 = bflo(xr0) + s0 * (f0 * __builtin_amdgcn_rcpf(1.f + __builtin_amdgcn_exp2f(-f0 * LOG2E)));
      const float y1 = bflo(xr1) + s1 * (f1 * __builtin_amdgcn_rcpf(1.f + __builtin_amdgcn_exp2f(-f1 * LOG2E)));
      if (lane < 4) {
        const u32 y = pk(y0, y1);
        ((GAS u16*)out)[(2 * bp) * 512 + 4 * cq + lane] = (u16)y;
        ((GAS u16*)out)[(2 * bp + 1) * 512 + 4 * cq + lane] = (u16)(y >> 16);
      }
      if (c == 0 && lane == 0) __hip_atomic_store((GAS u64*)ws, T, __ATOMIC_RELAXED, __HIP_MEMORY_SCOPE_AGENT);
      STAMP(10);
    }
    STAMP_FLUSH();
  }
}

extern "C" void kernel_launch(void* const* d_in, const int* in_sizes, int n_in, void* d_out, int out_size, void* d_ws, size_t ws_size,
                              hipStream_t stream) {
  fused_decode_kernel<<<256, 768, 0, stream>>>((const u16*)d_in[0], (const u16*)d_in[1], (const u16*)d_in[2], (const u16*)d_in[3],
                                               (const u16*)d_in[4], (const u16*)d_in[6], (const u16*)d_in[7], (const u16*)d_in[8],
                                               (u16*)d_out, d_ws);
}
```

```cpp
#define PROBE 4
#define PROBE_WG 0
#include <hip/hip_runtime.h>
#include <stdint.h>

namespace {
typedef unsigned short u16;
typedef unsigned u32;
typedef unsigned long long u64;
typedef u32 u32x4 __attribute__((ext_vector_type(4)));
typedef u32 u32x2 __attribute__((ext_vector_type(2)));
typedef float f32x4 __attribute__((ext_vector_type(4)));
typedef float f32x2 __attribute__((ext_vector_type(2)));
typedef short s16x4 __attribute__((ext_vector_type(4)));
typedef __bf16 bf16x2 __attribute__((ext_vector_type(2)));
typedef __bf16 bf16x8 __attribute__((ext_vector_type(8)));
#define LAS __attribute__((address_space(3)))
#define GAS __attribute__((address_space(1)))

constexpr u32 WS_EPOCH = 0;
constexpr u32 WS_Q = 4096;
constexpr u32 WS_G = WS_Q + 65536;
constexpr u32 WS_S = WS_G + 57344;
constexpr u32 WS_O = WS_S + 16384;
constexpr u32 WS_H = WS_O + 524288;
constexpr u32 WS_END = WS_H + 256 * 96 * 64;
constexpr u32 WS_DBG = (WS_END + 4095) & ~4095u;

constexpr u32 L_WQ = 0, L_WG = 16384, L_K = 32768, L_V = 65536, L_WO = 98304, L_W2 = 114688;
constexpr u32 L_XN = 122880, XN_S = 1088;
constexpr u32 L_QP = L_XN + 4 * XN_S;
constexpr u32 L_GP = L_QP + 2048;
constexpr u32 L_QI = L_GP + 2048, QI_S = 320;
constexpr u32 L_PI = L_QI + 4 * QI_S;
constexpr u32 L_MX = L_PI + 4 * QI_S;
constexpr u32 L_OA = L_MX + 128, OA_S = 192;
constexpr u32 L_HA = L_OA + 4 * OA_S;
constexpr u32 L_TAG = L_HA + 4 * OA_S;
constexpr u32 L_END = L_TAG + 16;

constexpr float SM_C = 0.08838834764831845f * 1.4426950408889634f;
constexpr float LOG2E = 1.4426950408889634f;
constexpr int SPIN_CAP = 1 << 16;
constexpr int AUX_SC1 = 16;

#define KV_POL 1
#define W_POL 1
#define POLSTR_0 ""
#define POLSTR_1 " nt"
#define POLSTR_2 " sc1"
#define POLSTR_3 " sc0 sc1 nt"
#define POLSTR_4 " sc1 nt"
#define POLSTR_5 " sc0 sc1"
#define POLCAT(x) POLSTR_##x
#define POLSTR(x) POLCAT(x)
#define KV_NT POLSTR(KV_POL)
#define W_NT POLSTR(W_POL)

__device__ __forceinline__ void dma_kv(const void* sbase, u32 voff, u32 lds) {
  asm volatile("s_mov_b32 m0, %2\n\ts_nop 4\n\tglobal_load_lds_dwordx4 %0, %1" KV_NT ::"v"(voff), "s"(sbase), "s"(lds) : "memory");
}
__device__ __forceinline__ void dma_w(const void* sbase, u32 voff, u32 lds) {
  asm volatile("s_mov_b32 m0, %2\n\ts_nop 4\n\tglobal_load_lds_dwordx4 %0, %1" W_NT ::"v"(voff), "s"(sbase), "s"(lds) : "memory");
}
#define VMWAIT(n) asm volatile("s_waitcnt vmcnt(" #n ")" ::: "memory")
#define BAR() asm volatile("s_waitcnt lgkmcnt(0)\n\ts_barrier" ::: "memory")

__device__ __forceinline__ u32 pk(float lo, float hi) {
  f32x2 v = {lo, hi};
  return __builtin_bit_cast(u32, __builtin_convertvector(v, bf16x2));
}
__device__ __forceinline__ float bflo(u32 v) { return __uint_as_float(v << 16); }
__device__ __forceinline__ float bfhi(u32 v) { return __uint_as_float(v & 0xffff0000u); }
__device__ __forceinline__ f32x4 mfma(u32x4 a, u32x4 b, f32x4 c) {
  return __builtin_amdgcn_mfma_f32_16x16x32_bf16(__builtin_bit_cast(bf16x8, a), __builtin_bit_cast(bf16x8, b), c, 0, 0, 0);
}
__device__ __forceinline__ u32x4 lds128(LAS unsigned char* L, u32 off) { return *(LAS u32x4*)(L + off); }
__device__ __forceinline__ u32x4 tr2(LAS unsigned char* L, u32 off0, u32 off1) {
  u32x2 a = __builtin_bit_cast(u32x2, __builtin_amdgcn_ds_read_tr16_b64_v4i16((LAS s16x4*)(L + off0)));
  u32x2 b = __builtin_bit_cast(u32x2, __builtin_amdgcn_ds_read_tr16_b64_v4i16((LAS s16x4*)(L + off1)));
  return u32x4{a.x, a.y, b.x, b.y};
}
template <int CTRL> __device__ __forceinline__ float dpp(float x) {
  return __builtin_bit_cast(float, __builtin_amdgcn_mov_dpp(__builtin_bit_cast(int, x), CTRL, 0xf, 0xf, true));
}
__device__ __forceinline__ float xrow_sum(float x) {
  auto s = __builtin_amdgcn_permlane16_swap(__float_as_uint(x), __float_as_uint(x), false, false);
  x = __uint_as_float(s[0]) + __uint_as_float(s[1]);
  auto t = __builtin_amdgcn_permlane32_swap(__float_as_uint(x), __float_as_uint(x), false, false);
  return __uint_as_float(t[0]) + __uint_as_float(t[1]);
}
__device__ __forceinline__ float xrow_max(float x) {
  auto s = __builtin_amdgcn_permlane16_swap(__float_as_uint(x), __float_as_uint(x), false, false);
  x = fmaxf(__uint_as_float(s[0]), __uint_as_float(s[1]));
  auto t = __builtin_amdgcn_permlane32_swap(__float_as_uint(x), __float_as_uint(x), false, false);
  return fmaxf(__uint_as_float(t[0]), __uint_as_float(t[1]));
}
__device__ __forceinline__ float wave_sum(float x) {
  x += dpp<0xB1>(x);
  x += dpp<0x4E>(x);
  x += dpp<0x141>(x);
  x += dpp<0x140>(x);
  return xrow_sum(x);
}
__device__ __forceinline__ u32x4 ldmb(__amdgpu_buffer_rsrc_t rs, u32 off) { return __builtin_amdgcn_raw_buffer_load_b128(rs, (int)off, 0, AUX_SC1); }
__device__ __forceinline__ void stmb(__amdgpu_buffer_rsrc_t rs, u32 off, u32x4 v) { __builtin_amdgcn_raw_buffer_store_b128(v, rs, (int)off, 0, AUX_SC1); }
__device__ __forceinline__ bool tagok(u32x4 v, u32 tA, u32 tB) { return v.y == tA && v.w == tB; }
__device__ __forceinline__ bool allok(bool ok) { return __builtin_amdgcn_ballot_w64(!ok) == 0ull; }

#define STAMP(k)
#define STAMP_DECL
#define STAMP_FLUSH()
}

__global__ void __launch_bounds__(768) fused_decode_kernel(const u16* __restrict__ xp, const u16* __restrict__ kc, const u16* __restrict__ vc,
                                                           const u16* __restrict__ wq, const u16* __restrict__ wo, const u16* __restrict__ w1,
                                                           const u16* __restrict__ w3, const u16* __restrict__ w2, u16* __restrict__ out,
                                                           void* __restrict__ ws) {
  __shared__ __attribute__((aligned(16))) unsigned char smem[L_END];
  LAS unsigned char* L = (LAS unsigned char*)smem;
  const u32 lb = (u32)(uintptr_t)L;
  const int tid = threadIdx.x, lane = tid & 63;
  const int w = __builtin_amdgcn_readfirstlane(tid >> 6);
  const int c = blockIdx.x, x = c & 7, j = c >> 3;
  const bool ffn = j < 28;
  const int n16 = lane & 15, g = lane >> 4;
  const __amdgpu_buffer_rsrc_t rs = __builtin_amdgcn_make_buffer_rsrc(ws, 0, (int)WS_END, 0x00020000);
  STAMP_DECL;
  STAMP(0);

  if (w < 8) {
    const int b = j >> 3, s = j & 7;
    const size_t kvb = ((size_t)((b * 8 + x) * 4096 + 3072 + 128 * s + 16 * w)) * 256;
    const char* kb = (const char*)kc + kvb;
    const char* vb = (const char*)vc + kvb;
    const int lg = 7 * x + (j >> 2), mtx = lg & 1, G1 = lg >> 1;
    {
      const char* sb = (const char*)wq + (size_t)(64 * w) * 8192 + (512 * x + 16 * j) * 2;
      const u32 vo = (u32)(lane >> 1) * 8192 + (lane & 1) * 16;
      dma_w(sb, vo, lb + L_WQ + (64 * w) * 32);
      dma_w(sb + 32 * 8192, vo, lb + L_WQ + (64 * w + 32) * 32);
    }
    if (ffn) {
      const char* sb = (const char*)(mtx ? w3 : w1) + (size_t)(64 * w) * 3584 + (64 * G1 + 16 * (j & 3)) * 2;
      const u32 vo = (u32)(lane >> 1) * 3584 + (lane & 1) * 16;
      dma_w(sb, vo, lb + L_WG + (64 * w) * 32);
      dma_w(sb + 32 * 3584, vo, lb + L_WG + (64 * w + 32) * 32);
    }
#pragma unroll
    for (int i = 0; i < 4; ++i) {
      const u32 r = 4 * i + g;
      dma_kv(kb, r * 256 + ((n16 ^ r) * 16), lb + L_K + (16 * w + 4 * i) * 256);
    }
#pragma unroll
    for (int i = 0; i < 4; ++i) {
      const u32 r = 4 * i + g;
      dma_kv(vb, r * 256 + ((n16 ^ ((g << 2) | i)) * 16), lb + L_V + (16 * w + 4 * i) * 256);
    }
    {
      const int dt = j >> 2, cg4 = j & 3;
      const char* sb = (const char*)wo + (size_t)((4 * x + (w >> 1)) * 128 + 16 * dt + 8 * (w & 1)) * 1024 + cg4 * 256;
#pragma unroll
      for (int i = 0; i < 2; ++i)
        dma_w(sb, (u32)(4 * i + g) * 1024 + ((n16 ^ ((g << 2) | ((2 * w + i) & 3))) * 16), lb + L_WO + (8 * w + 4 * i) * 256);
    }
    if (ffn) {
      const char* sb = (const char*)w2 + (size_t)(64 * j + 8 * w) * 1024 + x * 128;
      dma_w(sb, (u32)(lane >> 3) * 1024 + ((((lane & 7) >> 1) ^ (g & 3)) * 32) + (lane & 1) * 16, lb + L_W2 + 8 * w * 128);
    }
    STAMP(1);
    BAR();
    STAMP(2);
    const u32 tA = *(LAS u32*)(L + L_TAG), tB = *(LAS u32*)(L + L_TAG + 4);
    {
      const u32 xo = L_XN + (n16 & 3) * XN_S + g * 16 + 2 * w * 64;
      const u32x4 xa = lds128(L, xo), xb = lds128(L, xo + 64);
      if (ffn) VMWAIT(13); else VMWAIT(10);
      f32x4 acc = {0.f, 0.f, 0.f, 0.f};
      const u32 o = L_WQ + (64 * w) * 32 + lane * 8;
      acc = mfma(tr2(L, o, o + 512), xa, acc);
      acc = mfma(tr2(L, o + 1024, o + 1536), xb, acc);
      if (n16 < 4) *(LAS f32x4*)(L + L_QP + w * 256 + n16 * 64 + g * 16) = acc;
      if (ffn) {
        VMWAIT(11);
        f32x4 a2 = {0.f, 0.f, 0.f, 0.f};
        const u32 o2 = L_WG + (64 * w) * 32 + lane * 8;
        a2 = mfma(tr2(L, o2, o2 + 512), xa, a2);
        a2 = mfma(tr2(L, o2 + 1024, o2 + 1536), xb, a2);
        if (n16 < 4) *(LAS f32x4*)(L + L_GP + w * 256 + n16 * 64 + g * 16) = a2;
      }
    }
    STAMP(3);
    BAR();
    if (ffn) VMWAIT(7); else VMWAIT(6);
    STAMP(4);
    u32x4 kf[4];
#pragma unroll
    for (int st = 0; st < 4; ++st) kf[st] = lds128(L, L_K + (16 * w + n16) * 256 + (((4 * st + g) ^ n16) * 16));
    BAR();
    STAMP(5);
    f32x4 sc = {0.f, 0.f, 0.f, 0.f};
#pragma unroll
    for (int st = 0; st < 4; ++st) sc = mfma(kf[st], lds128(L, L_QI + (n16 & 3) * QI_S + st * 64 + g * 16), sc);
    float mx = xrow_max(fmaxf(fmaxf(sc.x, sc.y), fmaxf(sc.z, sc.w)));
    if (lane < 4) *(LAS float*)(L + L_MX + lane * 32 + w * 4) = mx;
    BAR();
    float Mc;
    {
      const f32x4 m0 = *(LAS f32x4*)(L + L_MX + (n16 & 3) * 32), m1 = *(LAS f32x4*)(L + L_MX + (n16 & 3) * 32 + 16);
      Mc = fmaxf(fmaxf(fmaxf(m0.x, m0.y), fmaxf(m0.z, m0.w)), fmaxf(fmaxf(m1.x, m1.y), fmaxf(m1.z, m1.w))) * SM_C;
      const float p0 = __builtin_amdgcn_exp2f(fmaf(sc.x, SM_C, -Mc)), p1 = __builtin_amdgcn_exp2f(fmaf(sc.y, SM_C, -Mc));
      const float p2 = __builtin_amdgcn_exp2f(fmaf(sc.z, SM_C, -Mc)), p3 = __builtin_amdgcn_exp2f(fmaf(sc.w, SM_C, -Mc));
      if (n16 < 4) *(LAS u32x2*)(L + L_PI + n16 * QI_S + (16 * w + 4 * g) * 2) = u32x2{pk(p0, p1), pk(p2, p3)};
    }
    if (ffn) VMWAIT(3); else VMWAIT(2);
    STAMP(6);
    BAR();
    {
      const int q4 = n16 >> 2, p4 = lane & 3;
      f32x4 o = {0.f, 0.f, 0.f, 0.f}, l = {0.f, 0.f, 0.f, 0.f};
      const u32x4 ones = {0x3F803F80u, 0x3F803F80u, 0x3F803F80u, 0x3F803F80u};
#pragma unroll
      for (int st = 0; st < 4; ++st) {
        const u32x4 pf = lds128(L, L_PI + (n16 & 3) * QI_S + st * 64 + g * 16);
        const u32 r0 = 32 * st + 8 * g + q4, r1 = r0 + 4;
        const u32 ch = 2 * w + (p4 >> 1);
        const u32 a0 = L_V + r0 * 256 + ((ch ^ ((q4 << 2) | ((2 * g) & 3))) * 16) + 8 * (p4 & 1);
        const u32 a1 = L_V + r1 * 256 + ((ch ^ ((q4 << 2) | ((2 * g + 1) & 3))) * 16) + 8 * (p4 & 1);
        o = mfma(pf, tr2(L, a0, a1), o);
        l = mfma(pf, ones, l);
      }
      if (lane < 16) stmb(rs, WS_O + (((x * 8 + w) * 32 + j) * 16 + lane) * 16, u32x4{pk(o.x, o.y), tA, pk(o.z, o.w), tB});
      if (w == 0 && lane < 4) {
        const float lv = lane == 0 ? l.x : lane == 1 ? l.y : lane == 2 ? l.z : l.w;
        stmb(rs, WS_S + ((x * 32 + j) * 4 + lane) * 16, u32x4{__float_as_uint(Mc), tA, __float_as_uint(lv), tB});
      }
    }
    STAMP(7);
    if (w == 0) VMWAIT(2); else VMWAIT(1);
    STAMP(8);
    BAR();
    STAMP(9);
    {
      const int q4 = n16 >> 2, p4 = lane & 3;
      f32x4 a = {0.f, 0.f, 0.f, 0.f};
#pragma unroll
      for (int st = 0; st < 2; ++st) {
        const u32x4 of = lds128(L, L_OA + (n16 & 3) * OA_S + st * 64 + g * 16);
        const u32 r0 = 32 * st + 8 * g + q4, r1 = r0 + 4;
        const u32 ch = 2 * w + (p4 >> 1);
        const u32 a0 = L_WO + r0 * 256 + ((ch ^ ((q4 << 2) | ((2 * g) & 3))) * 16) + 8 * (p4 & 1);
        const u32 a1 = L_WO + r1 * 256 + ((ch ^ ((q4 << 2) | ((2 * g + 1) & 3))) * 16) + 8 * (p4 & 1);
        a = mfma(of, tr2(L, a0, a1), a);
      }
      if (lane < 16) {
        const int cq = 32 * (j & 3) + 4 * w + (lane >> 2), slot = x * 8 + (j >> 2);
        const u32 o0 = WS_H + (((cq * 2) * 96 + slot) * 4 + (lane & 3)) * 16;
        stmb(rs, o0, u32x4{__float_as_uint(a.x), tA, __float_as_uint(a.y), tB});
        stmb(rs, o0 + 96 * 64, u32x4{__float_as_uint(a.z), tA, __float_as_uint(a.w), tB});
      }
      if (ffn && w < 4) {
        f32x4 f = {0.f, 0.f, 0.f, 0.f};
#pragma unroll
        for (int st = 0; st < 2; ++st) {
          const u32x4 hf = lds128(L, L_HA + (n16 & 3) * OA_S + st * 64 + g * 16);
          const u32 r0 = 32 * st + 4 * g + q4, r1 = r0 + 16;
          const u32 ph = (u32)(w ^ ((2 * g + (q4 >> 1)) & 3));
          f = mfma(hf, tr2(L, L_W2 + r0 * 128 + ph * 32 + 8 * p4, L_W2 + r1 * 128 + ph * 32 + 8 * p4), f);
        }
        if (lane < 16) {
          const int cq = 16 * x + 4 * w + (lane >> 2), slot = 64 + j;
          const u32 o0 = WS_H + (((cq * 2) * 96 + slot) * 4 + (lane & 3)) * 16;
          stmb(rs, o0, u32x4{__float_as_uint(f.x), tA, __float_as_uint(f.y), tB});
          stmb(rs, o0 + 96 * 64, u32x4{__float_as_uint(f.z), tA, __float_as_uint(f.w), tB});
        }
      }
    }
    STAMP(10);
    STAMP_FLUSH();
  } else {
    const int cb = w - 8;
    const u64 pr_t0 = __builtin_amdgcn_s_memrealtime(), pr_c0 = __builtin_amdgcn_s_memtime();
    u64 pr_b1 = 0, pr_b3 = 0, pr_b5 = 0, pr_b6 = 0, pr_b2 = 0;
    u64 E = 0;
    if (cb == 0) E = __hip_atomic_load((GAS u64*)ws, __ATOMIC_RELAXED, __HIP_MEMORY_SCOPE_AGENT);
    const u32x4 xv = *(const GAS u32x4*)(xp + cb * 512 + lane * 8);
    const int cq = c >> 1, bp = c & 1;
    u32 xr0 = 0, xr1 = 0;
    if (cb == 0) {
      xr0 = ((const GAS u16*)xp)[(2 * bp) * 512 + 4 * cq + (lane & 3)];
      xr1 = ((const GAS u16*)xp)[(2 * bp + 1) * 512 + 4 * cq + (lane & 3)];
    }
    {
      float v[8] = {bflo(xv.x), bfhi(xv.x), bflo(xv.y), bfhi(xv.y), bflo(xv.z), bfhi(xv.z), bflo(xv.w), bfhi(xv.w)};
      float sm = ((v[0] + v[1]) + (v[2] + v[3])) + ((v[4] + v[5]) + (v[6] + v[7]));
      const float mean = wave_sum(sm) * (1.0f / 512.0f);
      float sq = 0.f;
#pragma unroll
      for (int e = 0; e < 8; ++e) { v[e] -= mean; sq = fmaf(v[e], v[e], sq); }
      const float rstd = 1.0f / sqrtf(wave_sum(sq) * (1.0f / 512.0f) + 1e-5f);
      const u32 o = L_XN + cb * XN_S + (lane >> 2) * 64 + (lane & 1) * 32 + ((lane & 3) >> 1) * 8;
      *(LAS u32x2*)(L + o) = u32x2{pk(v[0] * rstd, v[1] * rstd), pk(v[2] * rstd, v[3] * rstd)};
      *(LAS u32x2*)(L + o + 16) = u32x2{pk(v[4] * rstd, v[5] * rstd), pk(v[6] * rstd, v[7] * rstd)};
    }
    u32 tA, tB;
    u64 T = 0;
    if (cb == 0) {
      T = E + 1;
      tA = (u32)T;
      tB = (u32)(E >> 32) ^ ((tA << 1) | 1u);
      if (lane == 0) *(LAS u32x2*)(L + L_TAG) = u32x2{tA, tB};
    }
    STAMP(1);
    BAR();
    pr_b1 = __builtin_amdgcn_s_memrealtime();
    STAMP(2);
    tA = *(LAS u32*)(L + L_TAG);
    tB = *(LAS u32*)(L + L_TAG + 4);
    BAR();
    STAMP(3);
    if (cb == 0) {
      f32x4 a = *(LAS f32x4*)(L + L_QP + lane * 16), a2 = *(LAS f32x4*)(L + L_QP + 1024 + lane * 16);
      a += a2;
      a.x = xrow_sum(a.x); a.y = xrow_sum(a.y); a.z = xrow_sum(a.z); a.w = xrow_sum(a.w);
      if (lane < 16) stmb(rs, WS_Q + ((x * 4 + (lane >> 2)) * 128 + 4 * j + (lane & 3)) * 16, u32x4{pk(a.x, a.y), tA, pk(a.z, a.w), tB});
      STAMP(4);
      const u32 qo = WS_Q + ((x * 4 + (j >> 3)) * 128 + lane) * 16;
      u32x4 g0, g1;
      for (int it = 0; it < SPIN_CAP; ++it) {
        asm volatile("" ::: "memory");
        g0 = ldmb(rs, qo);
        g1 = ldmb(rs, qo + 1024);
        if (allok(tagok(g0, tA, tB) && tagok(g1, tA, tB))) break;
      }
      *(LAS u32x2*)(L + L_QI + (lane >> 5) * QI_S + (lane & 31) * 8) = u32x2{g0.x, g0.z};
      *(LAS u32x2*)(L + L_QI + (2 + (lane >> 5)) * QI_S + (lane & 31) * 8) = u32x2{g1.x, g1.z};
      STAMP(5);
    } else if (cb == 1 && ffn) {
      f32x4 a = *(LAS f32x4*)(L + L_GP + lane * 16), a2 = *(LAS f32x4*)(L + L_GP + 1024 + lane * 16);
      a += a2;
      a.x = xrow_sum(a.x); a.y = xrow_sum(a.y); a.z = xrow_sum(a.z); a.w = xrow_sum(a.w);
      const int lg = 7 * x + (j >> 2);
      if (lane < 16) stmb(rs, WS_G + ((lg * 4 + (lane >> 2)) * 16 + 4 * (j & 3) + (lane & 3)) * 16, u32x4{pk(a.x, a.y), tA, pk(a.z, a.w), tB});
    }
    BAR();
    pr_b3 = __builtin_amdgcn_s_memrealtime();
    BAR();
    BAR();
    pr_b5 = __builtin_amdgcn_s_memrealtime();
    STAMP(6);
    {
      const u32 so = WS_S + ((x * 32 + 8 * cb + 2 * g) * 4 + (n16 & 3)) * 16;
      const u32 oo = WS_O + (((x * 8 + (j >> 2)) * 32 + 8 * cb + 2 * g) * 16 + n16) * 16;
      u32x4 sa, sb, da, db;
      if (cb == 1 && ffn) {
        const u32 go = WS_G + ((2 * j) * 64 + lane) * 16;
        u32x4 gg, uu;
        for (int it = 0; it < SPIN_CAP; ++it) {
          asm volatile("" ::: "memory");
          gg = ldmb(rs, go);
          uu = ldmb(rs, go + 1024);
          if (allok(tagok(gg, tA, tB) && tagok(uu, tA, tB))) break;
        }
        const float g0 = bflo(gg.x), g1 = bfhi(gg.x), g2 = bflo(gg.z), g3 = bfhi(gg.z);
        const float h0 = g0 * __builtin_amdgcn_rcpf(1.f + __builtin_amdgcn_exp2f(-g0 * LOG2E)) * bflo(uu.x);
        const float h1 = g1 * __builtin_amdgcn_rcpf(1.f + __builtin_amdgcn_exp2f(-g1 * LOG2E)) * bfhi(uu.x);
        const float h2 = g2 * __builtin_amdgcn_rcpf(1.f + __builtin_amdgcn_exp2f(-g2 * LOG2E)) * bflo(uu.z);
        const float h3 = g3 * __builtin_amdgcn_rcpf(1.f + __builtin_amdgcn_exp2f(-g3 * LOG2E)) * bfhi(uu.z);
        *(LAS u32x2*)(L + L_HA + g * OA_S + (n16 >> 3) * 64 + (n16 & 3) * 16 + ((n16 & 7) >> 2) * 8) = u32x2{pk(h0, h1), pk(h2, h3)};
      }
      for (int it = 0; it < SPIN_CAP; ++it) {
        asm volatile("" ::: "memory");
        sa = ldmb(rs, so); sb = ldmb(rs, so + 64);
        da = ldmb(rs, oo); db = ldmb(rs, oo + 256);
        if (allok(tagok(sa, tA, tB) && tagok(sb, tA, tB) && tagok(da, tA, tB) && tagok(db, tA, tB))) break;
      }
      STAMP(7);
      const float Ma = __uint_as_float(sa.x), La = __uint_as_float(sa.z), Mb = __uint_as_float(sb.x), Lb = __uint_as_float(sb.z);
      const float Mx = xrow_max(fmaxf(Ma, Mb));
      const float ea = __builtin_amdgcn_exp2f(Ma - Mx), eb = __builtin_amdgcn_exp2f(Mb - Mx);
      const float r = 1.0f / xrow_sum(fmaf(ea, La, eb * Lb));
      const u32 wp = pk(ea * r, eb * r);
      const u32 sh = (n16 & 1) * 16;
      const u32 A_ = (wp & 0xffffu) << sh, B_ = (wp >> 16) << sh;
      const bool lo = (n16 & 2) == 0;
      const u32x4 wf = {lo ? A_ : 0u, lo ? 0u : A_, lo ? B_ : 0u, lo ? 0u : B_};
      f32x4 d = {0.f, 0.f, 0.f, 0.f};
      d = mfma(u32x4{da.x, da.z, db.x, db.z}, wf, d);
      if (n16 < 4) *(LAS u32x2*)(L + L_OA + cb * OA_S + (16 * n16 + 4 * g) * 2) = u32x2{pk(d.x, d.y), pk(d.z, d.w)};
    }
    STAMP(8);
    BAR();
    pr_b6 = __builtin_amdgcn_s_memrealtime();
    if (cb == 0) {
      const u32 ho = WS_H + (c * 96 * 4 + lane) * 16;
      u32x4 h[6];
      const bool v5 = lane < 48;
      for (int it = 0; it < SPIN_CAP; ++it) {
        asm volatile("" ::: "memory");
        bool ok = true;
#pragma unroll
        for (int i = 0; i < 5; ++i) h[i] = ldmb(rs, ho + i * 1024);
        h[5] = ldmb(rs, ho + (v5 ? 5 : 4) * 1024);
#pragma unroll
        for (int i = 0; i < 6; ++i) ok = ok && tagok(h[i], tA, tB);
        if (allok(ok)) break;
      }
      STAMP(9);
      float s0 = 0.f, s1 = 0.f;
#pragma unroll
      for (int i = 0; i < 4; ++i) { s0 += __uint_as_float(h[i].x); s1 += __uint_as_float(h[i].z); }
      float f0 = __uint_as_float(h[4].x) + (v5 ? __uint_as_float(h[5].x) : 0.f);
      float f1 = __uint_as_float(h[4].z) + (v5 ? __uint_as_float(h[5].z) : 0.f);
      s0 += dpp<0x124>(s0); s1 += dpp<0x124>(s1); f0 += dpp<0x124>(f0); f1 += dpp<0x124>(f1);
      s0 += dpp<0x128>(s0); s1 += dpp<0x128>(s1); f0 += dpp<0x128>(f0); f1 += dpp<0x128>(f1);
      s0 = xrow_sum(s0); s1 = xrow_sum(s1); f0 = xrow_sum(f0); f1 = xrow_sum(f1);
      const float y0 = bflo(xr0) + s0 * (f0 * __builtin_amdgcn_rcpf(1.f + __builtin_amdgcn_exp2f(-f0 * LOG2E)));
      const float y1 = bflo(xr1) + s1 * (f1 * __builtin_amdgcn_rcpf(1.f + __builtin_amdgcn_exp2f(-f1 * LOG2E)));
      if (lane < 4) {
        const u32 y = pk(y0, y1);
        ((GAS u16*)out)[(2 * bp) * 512 + 4 * cq + lane] = (u16)y;
        ((GAS u16*)out)[(2 * bp + 1) * 512 + 4 * cq + lane] = (u16)(y >> 16);
      }
      if (c == 0 && lane == 0) __hip_atomic_store((GAS u64*)ws, T, __ATOMIC_RELAXED, __HIP_MEMORY_SCOPE_AGENT);
      if (c == PROBE_WG) {
        const u64 t1 = __builtin_amdgcn_s_memrealtime(), c1 = __builtin_amdgcn_s_memtime();
        u64 d = 0;
        if (PROBE == 1) d = (c1 - pr_c0) * 100 / (t1 - pr_t0);
        if (PROBE == 2) d = 2 * (t1 - pr_t0);
        if (PROBE == 3) d = 4 * (pr_b1 - pr_t0);
        if (PROBE == 4) d = 2 * (pr_b3 - pr_t0);
        if (PROBE == 5) d = 2 * (pr_b5 - pr_t0);
        if (PROBE == 6) d = 2 * (pr_b6 - pr_t0);
        if (d > 20000) d = 20000;
        while (__builtin_amdgcn_s_memrealtime() - t1 < d) __builtin_amdgcn_s_sleep(1);
      }
      STAMP(10);
    }
    STAMP_FLUSH();
  }
}

extern "C" void kernel_launch(void* const* d_in, const int* in_sizes, int n_in, void* d_out, int out_size, void* d_ws, size_t ws_size,
                              hipStream_t stream) {
  fused_decode_kernel<<<256, 768, 0, stream>>>((const u16*)d_in[0], (const u16*)d_in[1], (const u16*)d_in[2], (const u16*)d_in[3],
                                               (const u16*)d_in[4], (const u16*)d_in[6], (const u16*)d_in[7], (const u16*)d_in[8],
                                               (u16*)d_out, d_ws);
}
```

```cpp
#define PROBE 9
#define PROBE_WG 0
#include <hip/hip_runtime.h>
#include <stdint.h>

namespace {
typedef unsigned short u16;
typedef unsigned u32;
typedef unsigned long long u64;
typedef u32 u32x4 __attribute__((ext_vector_type(4)));
typedef u32 u32x2 __attribute__((ext_vector_type(2)));
typedef float f32x4 __attribute__((ext_vector_type(4)));
typedef float f32x2 __attribute__((ext_vector_type(2)));
typedef short s16x4 __attribute__((ext_vector_type(4)));
typedef __bf16 bf16x2 __attribute__((ext_vector_type(2)));
typedef __bf16 bf16x8 __attribute__((ext_vector_type(8)));
#define LAS __attribute__((address_space(3)))
#define GAS __attribute__((address_space(1)))

constexpr u32 WS_EPOCH = 0;
constexpr u32 WS_Q = 4096;
constexpr u32 WS_G = WS_Q + 65536;
constexpr u32 WS_S = WS_G + 57344;
constexpr u32 WS_O = WS_S + 16384;
constexpr u32 WS_H = WS_O + 524288;
constexpr u32 WS_END = WS_H + 256 * 96 * 64;
constexpr u32 WS_DBG = (WS_END + 4095) & ~4095u;

constexpr u32 L_WQ = 0, L_WG = 16384, L_K = 32768, L_V = 65536, L_WO = 98304, L_W2 = 114688;
constexpr u32 L_XN = 122880, XN_S = 1088;
constexpr u32 L_QP = L_XN + 4 * XN_S;
constexpr u32 L_GP = L_QP + 2048;
constexpr u32 L_QI = L_GP + 2048, QI_S = 320;
constexpr u32 L_PI = L_QI + 4 * QI_S;
constexpr u32 L_MX = L_PI + 4 * QI_S;
constexpr u32 L_OA = L_MX + 128, OA_S = 192;
constexpr u32 L_HA = L_OA + 4 * OA_S;
constexpr u32 L_TAG = L_HA + 4 * OA_S;
constexpr u32 L_END = L_TAG + 16;

constexpr float SM_C = 0.08838834764831845f * 1.4426950408889634f;
constexpr float LOG2E = 1.4426950408889634f;
constexpr int SPIN_CAP = 1 << 16;
constexpr int AUX_SC1 = 16;

#define KV_POL 1
#define W_POL 1
#define POLSTR_0 ""
#define POLSTR_1 " nt"
#define POLSTR_2 " sc1"
#define POLSTR_3 " sc0 sc1 nt"
#define POLSTR_4 " sc1 nt"
#define POLSTR_5 " sc0 sc1"
#define POLCAT(x) POLSTR_##x
#define POLSTR(x) POLCAT(x)
#define KV_NT POLSTR(KV_POL)
#define W_NT POLSTR(W_POL)

__device__ __forceinline__ void dma_kv(const void* sbase, u32 voff, u32 lds) {
  asm volatile("s_mov_b32 m0, %2\n\ts_nop 4\n\tglobal_load_lds_dwordx4 %0, %1" KV_NT ::"v"(voff), "s"(sbase), "s"(lds) : "memory");
}
__device__ __forceinline__ void dma_w(const void* sbase, u32 voff, u32 lds) {
  asm volatile("s_mov_b32 m0, %2\n\ts_nop 4\n\tglobal_load_lds_dwordx4 %0, %1" W_NT ::"v"(voff), "s"(sbase), "s"(lds) : "memory");
}
#define VMWAIT(n) asm volatile("s_waitcnt vmcnt(" #n ")" ::: "memory")
#define BAR() asm volatile("s_waitcnt lgkmcnt(0)\n\ts_barrier" ::: "memory")

__device__ __forceinline__ u32 pk(float lo, float hi) {
  f32x2 v = {lo, hi};
  return __builtin_bit_cast(u32, __builtin_convertvector(v, bf16x2));
}
__device__ __forceinline__ float bflo(u32 v) { return __uint_as_float(v << 16); }
__device__ __forceinline__ float bfhi(u32 v) { return __uint_as_float(v & 0xffff0000u); }
__device__ __forceinline__ f32x4 mfma(u32x4 a, u32x4 b, f32x4 c) {
  return __builtin_amdgcn_mfma_f32_16x16x32_bf16(__builtin_bit_cast(bf16x8, a), __builtin_bit_cast(bf16x8, b), c, 0, 0, 0);
}
__device__ __forceinline__ u32x4 lds128(LAS unsigned char* L, u32 off) { return *(LAS u32x4*)(L + off); }
__device__ __forceinline__ u32x4 tr2(LAS unsigned char* L, u32 off0, u32 off1) {
  u32x2 a = __builtin_bit_cast(u32x2, __builtin_amdgcn_ds_read_tr16_b64_v4i16((LAS s16x4*)(L + off0)));
  u32x2 b = __builtin_bit_cast(u32x2, __builtin_amdgcn_ds_read_tr16_b64_v4i16((LAS s16x4*)(L + off1)));
  return u32x4{a.x, a.y, b.x, b.y};
}
template <int CTRL> __device__ __forceinline__ float dpp(float x) {
  return __builtin_bit_cast(float, __builtin_amdgcn_mov_dpp(__builtin_bit_cast(int, x), CTRL, 0xf, 0xf, true));
}
__device__ __forceinline__ float xrow_sum(float x) {
  auto s = __builtin_amdgcn_permlane16_swap(__float_as_uint(x), __float_as_uint(x), false, false);
  x = __uint_as_float(s[0]) + __uint_as_float(s[1]);
  auto t = __builtin_amdgcn_permlane32_swap(__float_as_uint(x), __float_as_uint(x), false, false);
  return __uint_as_float(t[0]) + __uint_as_float(t[1]);
}
__device__ __forceinline__ float xrow_max(float x) {
  auto s = __builtin_amdgcn_permlane16_swap(__float_as_uint(x), __float_as_uint(x), false, false);
  x = fmaxf(__uint_as_float(s[0]), __uint_as_float(s[1]));
  auto t = __builtin_amdgcn_permlane32_swap(__float_as_uint(x), __float_as_uint(x), false, false);
  return fmaxf(__uint_as_float(t[0]), __uint_as_float(t[1]));
}
__device__ __forceinline__ float wave_sum(float x) {
  x += dpp<0xB1>(x);
  x += dpp<0x4E>(x);
  x += dpp<0x141>(x);
  x += dpp<0x140>(x);
  return xrow_sum(x);
}
__device__ __forceinline__ u32x4 ldmb(__amdgpu_buffer_rsrc_t rs, u32 off) { return __builtin_amdgcn_raw_buffer_load_b128(rs, (int)off, 0, AUX_SC1); }
__device__ __forceinline__ void stmb(__amdgpu_buffer_rsrc_t rs, u32 off, u32x4 v) { __builtin_amdgcn_raw_buffer_store_b128(v, rs, (int)off, 0, AUX_SC1); }
__device__ __forceinline__ bool tagok(u32x4 v, u32 tA, u32 tB) { return v.y == tA && v.w == tB; }
__device__ __forceinline__ bool allok(bool ok) { return __builtin_amdgcn_ballot_w64(!ok) == 0ull; }

#define STAMP(k)
#define STAMP_DECL
#define STAMP_FLUSH()
}

__global__ void __launch_bounds__(768) fused_decode_kernel(const u16* xp_, const u16* kc_, const u16* vc_,
                                                           const u16* wq_, const u16* wo_, const u16* w1_,
                                                           const u16* w3_, const u16* w2_, u16* out_,
                                                           void* ws_) {
  typedef const __attribute__((address_space(4))) u64* kargp;
  kargp ka = (kargp)__builtin_amdgcn_kernarg_segment_ptr();
  u64 pr_ta;
  asm volatile("s_memrealtime %0" : "=s"(pr_ta), "+s"(ka));
  const u16* xp = (const u16*)ka[0]; const u16* kc = (const u16*)ka[1]; const u16* vc = (const u16*)ka[2];
  const u16* wq = (const u16*)ka[3]; const u16* wo = (const u16*)ka[4]; const u16* w1 = (const u16*)ka[5];
  const u16* w3 = (const u16*)ka[6]; const u16* w2 = (const u16*)ka[7]; u16* out = (u16*)ka[8]; void* ws = (void*)ka[9];
  __shared__ __attribute__((aligned(16))) unsigned char smem[L_END];
  LAS unsigned char* L = (LAS unsigned char*)smem;
  const u32 lb = (u32)(uintptr_t)L;
  const int tid = threadIdx.x, lane = tid & 63;
  const int w = __builtin_amdgcn_readfirstlane(tid >> 6);
  const int c = blockIdx.x, x = c & 7, j = c >> 3;
  const bool ffn = j < 28;
  const int n16 = lane & 15, g = lane >> 4;
  const __amdgpu_buffer_rsrc_t rs = __builtin_amdgcn_make_buffer_rsrc(ws, 0, (int)WS_END, 0x00020000);
  STAMP_DECL;
  STAMP(0);

  if (w < 8) {
    const int b = j >> 3, s = j & 7;
    const size_t kvb = ((size_t)((b * 8 + x) * 4096 + 3072 + 128 * s + 16 * w)) * 256;
    const char* kb = (const char*)kc + kvb;
    const char* vb = (const char*)vc + kvb;
    const int lg = 7 * x + (j >> 2), mtx = lg & 1, G1 = lg >> 1;
    {
      const char* sb = (const char*)wq + (size_t)(64 * w) * 8192 + (512 * x + 16 * j) * 2;
      const u32 vo = (u32)(lane >> 1) * 8192 + (lane & 1) * 16;
      dma_w(sb, vo, lb + L_WQ + (64 * w) * 32);
      dma_w(sb + 32 * 8192, vo, lb + L_WQ + (64 * w + 32) * 32);
    }
    if (ffn) {
      const char* sb = (const char*)(mtx ? w3 : w1) + (size_t)(64 * w) * 3584 + (64 * G1 + 16 * (j & 3)) * 2;
      const u32 vo = (u32)(lane >> 1) * 3584 + (lane & 1) * 16;
      dma_w(sb, vo, lb + L_WG + (64 * w) * 32);
      dma_w(sb + 32 * 3584, vo, lb + L_WG + (64 * w + 32) * 32);
    }
#pragma unroll
    for (int i = 0; i < 4; ++i) {
      const u32 r = 4 * i + g;
      dma_kv(kb, r * 256 + ((n16 ^ r) * 16), lb + L_K + (16 * w + 4 * i) * 256);
    }
#pragma unroll
    for (int i = 0; i < 4; ++i) {
      const u32 r = 4 * i + g;
      dma_kv(vb, r * 256 + ((n16 ^ ((g << 2) | i)) * 16), lb + L_V + (16 * w + 4 * i) * 256);
    }
    {
      const int dt = j >> 2, cg4 = j & 3;
      const char* sb = (const char*)wo + (size_t)((4 * x + (w >> 1)) * 128 + 16 * dt + 8 * (w & 1)) * 1024 + cg4 * 256;
#pragma unroll
      for (int i = 0; i < 2; ++i)
        dma_w(sb, (u32)(4 * i + g) * 1024 + ((n16 ^ ((g << 2) | ((2 * w + i) & 3))) * 16), lb + L_WO + (8 * w + 4 * i) * 256);
    }
    if (ffn) {
      const char* sb = (const char*)w2 + (size_t)(64 * j + 8 * w) * 1024 + x * 128;
      dma_w(sb, (u32)(lane >> 3) * 1024 + ((((lane & 7) >> 1) ^ (g & 3)) * 32) + (lane & 1) * 16, lb + L_W2 + 8 * w * 128);
    }
    STAMP(1);
    BAR();
    STAMP(2);
    const u32 tA = *(LAS u32*)(L + L_TAG), tB = *(LAS u32*)(L + L_TAG + 4);
    {
      const u32 xo = L_XN + (n16 & 3) * XN_S + g * 16 + 2 * w * 64;
      const u32x4 xa = lds128(L, xo), xb = lds128(L, xo + 64);
      if (ffn) VMWAIT(13); else VMWAIT(10);
      f32x4 acc = {0.f, 0.f, 0.f, 0.f};
      const u32 o = L_WQ + (64 * w) * 32 + lane * 8;
      acc = mfma(tr2(L, o, o + 512), xa, acc);
      acc = mfma(tr2(L, o + 1024, o + 1536), xb, acc);
      if (n16 < 4) *(LAS f32x4*)(L + L_QP + w * 256 + n16 * 64 + g * 16) = acc;
      if (ffn) {
        VMWAIT(11);
        f32x4 a2 = {0.f, 0.f, 0.f, 0.f};
        const u32 o2 = L_WG + (64 * w) * 32 + lane * 8;
        a2 = mfma(tr2(L, o2, o2 + 512), xa, a2);
        a2 = mfma(tr2(L, o2 + 1024, o2 + 1536), xb, a2);
        if (n16 < 4) *(LAS f32x4*)(L + L_GP + w * 256 + n16 * 64 + g * 16) = a2;
      }
    }
    STAMP(3);
    BAR();
    if (ffn) VMWAIT(7); else VMWAIT(6);
    STAMP(4);
    u32x4 kf[4];
#pragma unroll
    for (int st = 0; st < 4; ++st) kf[st] = lds128(L, L_K + (16 * w + n16) * 256 + (((4 * st + g) ^ n16) * 16));
    BAR();
    STAMP(5);
    f32x4 sc = {0.f, 0.f, 0.f, 0.f};
#pragma unroll
    for (int st = 0; st < 4; ++st) sc = mfma(kf[st], lds128(L, L_QI + (n16 & 3) * QI_S + st * 64 + g * 16), sc);
    float mx = xrow_max(fmaxf(fmaxf(sc.x, sc.y), fmaxf(sc.z, sc.w)));
    if (lane < 4) *(LAS float*)(L + L_MX + lane * 32 + w * 4) = mx;
    BAR();
    float Mc;
    {
      const f32x4 m0 = *(LAS f32x4*)(L + L_MX + (n16 & 3) * 32), m1 = *(LAS f32x4*)(L + L_MX + (n16 & 3) * 32 + 16);
      Mc = fmaxf(fmaxf(fmaxf(m0.x, m0.y), fmaxf(m0.z, m0.w)), fmaxf(fmaxf(m1.x, m1.y), fmaxf(m1.z, m1.w))) * SM_C;
      const float p0 = __builtin_amdgcn_exp2f(fmaf(sc.x, SM_C, -Mc)), p1 = __builtin_amdgcn_exp2f(fmaf(sc.y, SM_C, -Mc));
      const float p2 = __builtin_amdgcn_exp2f(fmaf(sc.z, SM_C, -Mc)), p3 = __builtin_amdgcn_exp2f(fmaf(sc.w, SM_C, -Mc));
      if (n16 < 4) *(LAS u32x2*)(L + L_PI + n16 * QI_S + (16 * w + 4 * g) * 2) = u32x2{pk(p0, p1), pk(p2, p3)};
    }
    if (ffn) VMWAIT(3); else VMWAIT(2);
    STAMP(6);
    BAR();
    {
      const int q4 = n16 >> 2, p4 = lane & 3;
      f32x4 o = {0.f, 0.f, 0.f, 0.f}, l = {0.f, 0.f, 0.f, 0.f};
      const u32x4 ones = {0x3F803F80u, 0x3F803F80u, 0x3F803F80u, 0x3F803F80u};
#pragma unroll
      for (int st = 0; st < 4; ++st) {
        const u32x4 pf = lds128(L, L_PI + (n16 & 3) * QI_S + st * 64 + g * 16);
        const u32 r0 = 32 * st + 8 * g + q4, r1 = r0 + 4;
        const u32 ch = 2 * w + (p4 >> 1);
        const u32 a0 = L_V + r0 * 256 + ((ch ^ ((q4 << 2) | ((2 * g) & 3))) * 16) + 8 * (p4 & 1);
        const u32 a1 = L_V + r1 * 256 + ((ch ^ ((q4 << 2) | ((2 * g + 1) & 3))) * 16) + 8 * (p4 & 1);
        o = mfma(pf, tr2(L, a0, a1), o);
        l = mfma(pf, ones, l);
      }
      if (lane < 16) stmb(rs, WS_O + (((x * 8 + w) * 32 + j) * 16 + lane) * 16, u32x4{pk(o.x, o.y), tA, pk(o.z, o.w), tB});
      if (w == 0 && lane < 4) {
        const float lv = lane == 0 ? l.x : lane == 1 ? l.y : lane == 2 ? l.z : l.w;
        stmb(rs, WS_S + ((x * 32 + j) * 4 + lane) * 16, u32x4{__float_as_uint(Mc), tA, __float_as_uint(lv), tB});
      }
    }
    STAMP(7);
    if (w == 0) VMWAIT(2); else VMWAIT(1);
    STAMP(8);
    BAR();
    STAMP(9);
    {
      const int q4 = n16 >> 2, p4 = lane & 3;
      f32x4 a = {0.f, 0.f, 0.f, 0.f};
#pragma unroll
      for (int st = 0; st < 2; ++st) {
        const u32x4 of = lds128(L, L_OA + (n16 & 3) * OA_S + st * 64 + g * 16);
        const u32 r0 = 32 * st + 8 * g + q4, r1 = r0 + 4;
        const u32 ch = 2 * w + (p4 >> 1);
        const u32 a0 = L_WO + r0 * 256 + ((ch ^ ((q4 << 2) | ((2 * g) & 3))) * 16) + 8 * (p4 & 1);
        const u32 a1 = L_WO + r1 * 256 + ((ch ^ ((q4 << 2) | ((2 * g + 1) & 3))) * 16) + 8 * (p4 & 1);
        a = mfma(of, tr2(L, a0, a1), a);
      }
      if (lane < 16) {
        const int cq = 32 * (j & 3) + 4 * w + (lane >> 2), slot = x * 8 + (j >> 2);
        const u32 o0 = WS_H + (((cq * 2) * 96 + slot) * 4 + (lane & 3)) * 16;
        stmb(rs, o0, u32x4{__float_as_uint(a.x), tA, __float_as_uint(a.y), tB});
        stmb(rs, o0 + 96 * 64, u32x4{__float_as_uint(a.z), tA, __float_as_uint(a.w), tB});
      }
      if (ffn && w < 4) {
        f32x4 f = {0.f, 0.f, 0.f, 0.f};
#pragma unroll
        for (int st = 0; st < 2; ++st) {
          const u32x4 hf = lds128(L, L_HA + (n16 & 3) * OA_S + st * 64 + g * 16);
          const u32 r0 = 32 * st + 4 * g + q4, r1 = r0 + 16;
          const u32 ph = (u32)(w ^ ((2 * g + (q4 >> 1)) & 3));
          f = mfma(hf, tr2(L, L_W2 + r0 * 128 + ph * 32 + 8 * p4, L_W2 + r1 * 128 + ph * 32 + 8 * p4), f);
        }
        if (lane < 16) {
          const int cq = 16 * x + 4 * w + (lane >> 2), slot = 64 + j;
          const u32 o0 = WS_H + (((cq * 2) * 96 + slot) * 4 + (lane & 3)) * 16;
          stmb(rs, o0, u32x4{__float_as_uint(f.x), tA, __float_as_uint(f.y), tB});
          stmb(rs, o0 + 96 * 64, u32x4{__float_as_uint(f.z), tA, __float_as_uint(f.w), tB});
        }
      }
    }
    STAMP(10);
    STAMP_FLUSH();
  } else {
    const int cb = w - 8;
    const u64 pr_t0 = __builtin_amdgcn_s_memrealtime(), pr_c0 = __builtin_amdgcn_s_memtime();
    u64 pr_b1 = 0, pr_b3 = 0, pr_b5 = 0, pr_b6 = 0, pr_b2 = 0;
    u64 E = 0;
    if (cb == 0) E = __hip_atomic_load((GAS u64*)ws, __ATOMIC_RELAXED, __HIP_MEMORY_SCOPE_AGENT);
    const u32x4 xv = *(const GAS u32x4*)(xp + cb * 512 + lane * 8);
    const int cq = c >> 1, bp = c & 1;
    u32 xr0 = 0, xr1 = 0;
    if (cb == 0) {
      xr0 = ((const GAS u16*)xp)[(2 * bp) * 512 + 4 * cq + (lane & 3)];
      xr1 = ((const GAS u16*)xp)[(2 * bp + 1) * 512 + 4 * cq + (lane & 3)];
    }
    {
      float v[8] = {bflo(xv.x), bfhi(xv.x), bflo(xv.y), bfhi(xv.y), bflo(xv.z), bfhi(xv.z), bflo(xv.w), bfhi(xv.w)};
      float sm = ((v[0] + v[1]) + (v[2] + v[3])) + ((v[4] + v[5]) + (v[6] + v[7]));
      const float mean = wave_sum(sm) * (1.0f / 512.0f);
      float sq = 0.f;
#pragma unroll
      for (int e = 0; e < 8; ++e) { v[e] -= mean; sq = fmaf(v[e], v[e], sq); }
      const float rstd = 1.0f / sqrtf(wave_sum(sq) * (1.0f / 512.0f) + 1e-5f);
      const u32 o = L_XN + cb * XN_S + (lane >> 2) * 64 + (lane & 1) * 32 + ((lane & 3) >> 1) * 8;
      *(LAS u32x2*)(L + o) = u32x2{pk(v[0] * rstd, v[1] * rstd), pk(v[2] * rstd, v[3] * rstd)};
      *(LAS u32x2*)(L + o + 16) = u32x2{pk(v[4] * rstd, v[5] * rstd), pk(v[6] * rstd, v[7] * rstd)};
    }
    u32 tA, tB;
    u64 T = 0;
    if (cb == 0) {
      T = E + 1;
      tA = (u32)T;
      tB = (u32)(E >> 32) ^ ((tA << 1) | 1u);
      if (lane == 0) *(LAS u32x2*)(L + L_TAG) = u32x2{tA, tB};
    }
    STAMP(1);
    BAR();
    pr_b1 = __builtin_amdgcn_s_memrealtime();
    STAMP(2);
    tA = *(LAS u32*)(L + L_TAG);
    tB = *(LAS u32*)(L + L_TAG + 4);
    asm volatile("s_waitcnt lgkmcnt(0)" : "+s"(pr_ta));
    if (cb == 0 && lane == 0) __hip_atomic_store((GAS u64*)((char*)ws + WS_DBG) + c, ((u64)tA << 32) | (u32)pr_ta, __ATOMIC_RELAXED, __HIP_MEMORY_SCOPE_AGENT);
    BAR();
    STAMP(3);
    if (cb == 0) {
      f32x4 a = *(LAS f32x4*)(L + L_QP + lane * 16), a2 = *(LAS f32x4*)(L + L_QP + 1024 + lane * 16);
      a += a2;
      a.x = xrow_sum(a.x); a.y = xrow_sum(a.y); a.z = xrow_sum(a.z); a.w = xrow_sum(a.w);
      if (lane < 16) stmb(rs, WS_Q + ((x * 4 + (lane >> 2)) * 128 + 4 * j + (lane & 3)) * 16, u32x4{pk(a.x, a.y), tA, pk(a.z, a.w), tB});
      STAMP(4);
      const u32 qo = WS_Q + ((x * 4 + (j >> 3)) * 128 + lane) * 16;
      u32x4 g0, g1;
      for (int it = 0; it < SPIN_CAP; ++it) {
        asm volatile("" ::: "memory");
        g0 = ldmb(rs, qo);
        g1 = ldmb(rs, qo + 1024);
        if (allok(tagok(g0, tA, tB) && tagok(g1, tA, tB))) break;
      }
      *(LAS u32x2*)(L + L_QI + (lane >> 5) * QI_S + (lane & 31) * 8) = u32x2{g0.x, g0.z};
      *(LAS u32x2*)(L + L_QI + (2 + (lane >> 5)) * QI_S + (lane & 31) * 8) = u32x2{g1.x, g1.z};
      STAMP(5);
    } else if (cb == 1 && ffn) {
      f32x4 a = *(LAS f32x4*)(L + L_GP + lane * 16), a2 = *(LAS f32x4*)(L + L_GP + 1024 + lane * 16);
      a += a2;
      a.x = xrow_sum(a.x); a.y = xrow_sum(a.y); a.z = xrow_sum(a.z); a.w = xrow_sum(a.w);
      const int lg = 7 * x + (j >> 2);
      if (lane < 16) stmb(rs, WS_G + ((lg * 4 + (lane >> 2)) * 16 + 4 * (j & 3) + (lane & 3)) * 16, u32x4{pk(a.x, a.y), tA, pk(a.z, a.w), tB});
    }
    BAR();
    pr_b3 = __builtin_amdgcn_s_memrealtime();
    BAR();
    BAR();
    pr_b5 = __builtin_amdgcn_s_memrealtime();
    STAMP(6);
    {
      const u32 so = WS_S + ((x * 32 + 8 * cb + 2 * g) * 4 + (n16 & 3)) * 16;
      const u32 oo = WS_O + (((x * 8 + (j >> 2)) * 32 + 8 * cb + 2 * g) * 16 + n16) * 16;
      u32x4 sa, sb, da, db;
      if (cb == 1 && ffn) {
        const u32 go = WS_G + ((2 * j) * 64 + lane) * 16;
        u32x4 gg, uu;
        for (int it = 0; it < SPIN_CAP; ++it) {
          asm volatile("" ::: "memory");
          gg = ldmb(rs, go);
          uu = ldmb(rs, go + 1024);
          if (allok(tagok(gg, tA, tB) && tagok(uu, tA, tB))) break;
        }
        const float g0 = bflo(gg.x), g1 = bfhi(gg.x), g2 = bflo(gg.z), g3 = bfhi(gg.z);
        const float h0 = g0 * __builtin_amdgcn_rcpf(1.f + __builtin_amdgcn_exp2f(-g0 * LOG2E)) * bflo(uu.x);
        const float h1 = g1 * __builtin_amdgcn_rcpf(1.f + __builtin_amdgcn_exp2f(-g1 * LOG2E)) * bfhi(uu.x);
        const float h2 = g2 * __builtin_amdgcn_rcpf(1.f + __builtin_amdgcn_exp2f(-g2 * LOG2E)) * bflo(uu.z);
        const float h3 = g3 * __builtin_amdgcn_rcpf(1.f + __builtin_amdgcn_exp2f(-g3 * LOG2E)) * bfhi(uu.z);
        *(LAS u32x2*)(L + L_HA + g * OA_S + (n16 >> 3) * 64 + (n16 & 3) * 16 + ((n16 & 7) >> 2) * 8) = u32x2{pk(h0, h1), pk(h2, h3)};
      }
      for (int it = 0; it < SPIN_CAP; ++it) {
        asm volatile("" ::: "memory");
        sa = ldmb(rs, so); sb = ldmb(rs, so + 64);
        da = ldmb(rs, oo); db = ldmb(rs, oo + 256);
        if (allok(tagok(sa, tA, tB) && tagok(sb, tA, tB) && tagok(da, tA, tB) && tagok(db, tA, tB))) break;
      }
      STAMP(7);
      const float Ma = __uint_as_float(sa.x), La = __uint_as_float(sa.z), Mb = __uint_as_float(sb.x), Lb = __uint_as_float(sb.z);
      const float Mx = xrow_max(fmaxf(Ma, Mb));
      const float ea = __builtin_amdgcn_exp2f(Ma - Mx), eb = __builtin_amdgcn_exp2f(Mb - Mx);
      const float r = 1.0f / xrow_sum(fmaf(ea, La, eb * Lb));
      const u32 wp = pk(ea * r, eb * r);
      const u32 sh = (n16 & 1) * 16;
      const u32 A_ = (wp & 0xffffu) << sh, B_ = (wp >> 16) << sh;
      const bool lo = (n16 & 2) == 0;
      const u32x4 wf = {lo ? A_ : 0u, lo ? 0u : A_, lo ? B_ : 0u, lo ? 0u : B_};
      f32x4 d = {0.f, 0.f, 0.f, 0.f};
      d = mfma(u32x4{da.x, da.z, db.x, db.z}, wf, d);
      if (n16 < 4) *(LAS u32x2*)(L + L_OA + cb * OA_S + (16 * n16 + 4 * g) * 2) = u32x2{pk(d.x, d.y), pk(d.z, d.w)};
    }
    STAMP(8);
    BAR();
    pr_b6 = __builtin_amdgcn_s_memrealtime();
    if (cb == 0) {
      const u32 ho = WS_H + (c * 96 * 4 + lane) * 16;
      u32x4 h[6];
      const bool v5 = lane < 48;
      for (int it = 0; it < SPIN_CAP; ++it) {
        asm volatile("" ::: "memory");
        bool ok = true;
#pragma unroll
        for (int i = 0; i < 5; ++i) h[i] = ldmb(rs, ho + i * 1024);
        h[5] = ldmb(rs, ho + (v5 ? 5 : 4) * 1024);
#pragma unroll
        for (int i = 0; i < 6; ++i) ok = ok && tagok(h[i], tA, tB);
        if (allok(ok)) break;
      }
      STAMP(9);
      float s0 = 0.f, s1 = 0.f;
#pragma unroll
      for (int i = 0; i < 4; ++i) { s0 += __uint_as_float(h[i].x); s1 += __uint_as_float(h[i].z); }
      float f0 = __uint_as_float(h[4].x) + (v5 ? __uint_as_float(h[5].x) : 0.f);
      float f1 = __uint_as_float(h[4].z) + (v5 ? __uint_as_float(h[5].z) : 0.f);
      s0 += dpp<0x124>(s0); s1 += dpp<0x124>(s1); f0 += dpp<0x124>(f0); f1 += dpp<0x124>(f1);
      s0 += dpp<0x128>(s0); s1 += dpp<0x128>(s1); f0 += dpp<0x128>(f0); f1 += dpp<0x128>(f1);
      s0 = xrow_sum(s0); s1 = xrow_sum(s1); f0 = xrow_sum(f0); f1 = xrow_sum(f1);
      const float y0 = bflo(xr0) + s0 * (f0 * __builtin_amdgcn_rcpf(1.f + __builtin_amdgcn_exp2f(-f0 * LOG2E)));
      const float y1 = bflo(xr1) + s1 * (f1 * __builtin_amdgcn_rcpf(1.f + __builtin_amdgcn_exp2f(-f1 * LOG2E)));
      if (lane < 4) {
        const u32 y = pk(y0, y1);
        ((GAS u16*)out)[(2 * bp) * 512 + 4 * cq + lane] = (u16)y;
        ((GAS u16*)out)[(2 * bp + 1) * 512 + 4 * cq + lane] = (u16)(y >> 16);
      }
      if (c == 0 && lane == 0) __hip_atomic_store((GAS u64*)ws, T, __ATOMIC_RELAXED, __HIP_MEMORY_SCOPE_AGENT);
      if (PROBE == 11 && c != PROBE_WG && lane == 0) __hip_atomic_store((GAS u64*)((char*)ws + WS_DBG + 4096) + c, ((u64)tA << 32) | (u32)__builtin_amdgcn_s_memrealtime(), __ATOMIC_RELAXED, __HIP_MEMORY_SCOPE_AGENT);
      if (c == PROBE_WG) {
        const u64 t1 = __builtin_amdgcn_s_memrealtime(), c1 = __builtin_amdgcn_s_memtime();
        u64 d = 0;
        if (PROBE == 1) d = (c1 - pr_c0) * 100 / (t1 - pr_t0);
        if (PROBE == 2) d = 2 * (t1 - pr_t0);
        if (PROBE == 3) d = 4 * (pr_b1 - pr_t0);
        if (PROBE == 4) d = 2 * (pr_b3 - pr_t0);
        if (PROBE == 5) d = 2 * (pr_b5 - pr_t0);
        if (PROBE == 6) d = 2 * (pr_b6 - pr_t0);
        if (PROBE == 7) d = 8 * (pr_t0 - pr_ta);
        if (PROBE >= 8 && PROBE <= 10) {
          u32 mn = 0xffffffffu, mx = 0;
          for (int i = 0; i < 4; ++i) { u64 v = __hip_atomic_load((GAS u64*)((char*)ws + WS_DBG) + i * 64 + lane, __ATOMIC_RELAXED, __HIP_MEMORY_SCOPE_AGENT);
            u32 tt = (u32)v; if ((u32)(v >> 32) == tA) { mn = min(mn, tt); mx = max(mx, tt); } }
          for (int o = 32; o > 0; o >>= 1) { mn = min(mn, (u32)__shfl_xor((int)mn, o)); mx = max(mx, (u32)__shfl_xor((int)mx, o)); }
          if (PROBE == 8) d = 4 * (u64)(mx - mn);
          if (PROBE == 9) d = 4 * (u64)((u32)pr_ta - mn);
          if (PROBE == 10) d = 2 * (u64)((u32)t1 - mn);
          d = __builtin_amdgcn_readfirstlane((u32)d);
        }
        if (PROBE == 11) {
          u32 mx = 0;
          for (int it = 0; it < 100000; ++it) { bool ok = true; mx = 0;
            for (int i = 0; i < 4; ++i) { u64 v = __hip_atomic_load((GAS u64*)((char*)ws + WS_DBG + 4096) + i * 64 + lane, __ATOMIC_RELAXED, __HIP_MEMORY_SCOPE_AGENT);
              if (i * 64 + lane != PROBE_WG) { ok = ok && ((u32)(v >> 32) == tA); mx = max(mx, (u32)v - (u32)pr_t0); } }
            if (allok(ok)) break; }
          for (int o = 32; o > 0; o >>= 1) mx = max(mx, (u32)__shfl_xor((int)mx, o));
          d = 2 * (u64)mx;
          d = __builtin_amdgcn_readfirstlane((u32)d);
        }
        if (d > 20000) d = 20000;
        while (__builtin_amdgcn_s_memrealtime() - t1 < d) __builtin_amdgcn_s_sleep(1);
      }
      STAMP(10);
    }
    STAMP_FLUSH();
  }
}

extern "C" void kernel_launch(void* const* d_in, const int* in_sizes, int n_in, void* d_out, int out_size, void* d_ws, size_t ws_size,
                              hipStream_t stream) {
  fused_decode_kernel<<<256, 768, 0, stream>>>((const u16*)d_in[0], (const u16*)d_in[1], (const u16*)d_in[2], (const u16*)d_in[3],
                                               (const u16*)d_in[4], (const u16*)d_in[6], (const u16*)d_in[7], (const u16*)d_in[8],
                                               (u16*)d_out, d_ws);
}
```

```cpp
#include <hip/hip_runtime.h>
#include <stdint.h>

namespace {
typedef unsigned short u16;
typedef unsigned u32;
typedef unsigned long long u64;
typedef u32 u32x4 __attribute__((ext_vector_type(4)));
typedef u32 u32x2 __attribute__((ext_vector_type(2)));
typedef float f32x4 __attribute__((ext_vector_type(4)));
typedef float f32x2 __attribute__((ext_vector_type(2)));
typedef short s16x4 __attribute__((ext_vector_type(4)));
typedef __bf16 bf16x2 __attribute__((ext_vector_type(2)));
typedef __bf16 bf16x8 __attribute__((ext_vector_type(8)));
#define LAS __attribute__((address_space(3)))
#define GAS __attribute__((address_space(1)))

constexpr u32 WS_EPOCH = 0;
constexpr u32 WS_Q = 4096;
constexpr u32 WS_G = WS_Q + 65536;
constexpr u32 WS_S = WS_G + 57344;
constexpr u32 WS_O = WS_S + 16384;
constexpr u32 WS_H = WS_O + 524288;
constexpr u32 WS_END = WS_H + 256 * 96 * 64;
constexpr u32 WS_DBG = (WS_END + 4095) & ~4095u;

constexpr u32 L_WQ = 0, L_WG = 16384, L_K = 32768, L_V = 65536, L_WO = 98304, L_W2 = 114688;
constexpr u32 L_XN = 122880, XN_S = 1088;
constexpr u32 L_QP = L_XN + 4 * XN_S;
constexpr u32 L_GP = L_QP + 2048;
constexpr u32 L_QI = L_GP + 2048, QI_S = 320;
constexpr u32 L_PI = L_QI + 4 * QI_S;
constexpr u32 L_MX = L_PI + 4 * QI_S;
constexpr u32 L_OA = L_MX + 128, OA_S = 192;
constexpr u32 L_HA = L_OA + 4 * OA_S;
constexpr u32 L_TAG = L_HA + 4 * OA_S;
constexpr u32 L_END = L_TAG + 16;

constexpr float SM_C = 0.08838834764831845f * 1.4426950408889634f;
constexpr float LOG2E = 1.4426950408889634f;
constexpr int SPIN_CAP = 1 << 16;
constexpr int AUX_SC1 = 16;

#define KV_POL 0
#define W_POL 0
#define POLSTR_0 ""
#define POLSTR_1 " nt"
#define POLSTR_2 " sc1"
#define POLSTR_3 " sc0 sc1 nt"
#define POLSTR_4 " sc1 nt"
#define POLSTR_5 " sc0 sc1"
#define POLCAT(x) POLSTR_##x
#define POLSTR(x) POLCAT(x)
#define KV_NT POLSTR(KV_POL)
#define W_NT POLSTR(W_POL)

__device__ __forceinline__ void dma_kv(const void* sbase, u32 voff, u32 lds) {
  asm volatile("s_mov_b32 m0, %2\n\ts_nop 4\n\tglobal_load_lds_dwordx4 %0, %1" KV_NT ::"v"(voff), "s"(sbase), "s"(lds) : "memory");
}
__device__ __forceinline__ void dma_w(const void* sbase, u32 voff, u32 lds) {
  asm volatile("s_mov_b32 m0, %2\n\ts_nop 4\n\tglobal_load_lds_dwordx4 %0, %1" W_NT ::"v"(voff), "s"(sbase), "s"(lds) : "memory");
}
#define VMWAIT(n) asm volatile("s_waitcnt vmcnt(" #n ")" ::: "memory")
#define BAR() asm volatile("s_waitcnt lgkmcnt(0)\n\ts_barrier" ::: "memory")

__device__ __forceinline__ u32 pk(float lo, float hi) {
  f32x2 v = {lo, hi};
  return __builtin_bit_cast(u32, __builtin_convertvector(v, bf16x2));
}
__device__ __forceinline__ float bflo(u32 v) { return __uint_as_float(v << 16); }
__device__ __forceinline__ float bfhi(u32 v) { return __uint_as_float(v & 0xffff0000u); }
__device__ __forceinline__ f32x4 mfma(u32x4 a, u32x4 b, f32x4 c) {
  return __builtin_amdgcn_mfma_f32_16x16x32_bf16(__builtin_bit_cast(bf16x8, a), __builtin_bit_cast(bf16x8, b), c, 0, 0, 0);
}
__device__ __forceinline__ u32x4 lds128(LAS unsigned char* L, u32 off) { return *(LAS u32x4*)(L + off); }
__device__ __forceinline__ u32x4 tr2(LAS unsigned char* L, u32 off0, u32 off1) {
  u32x2 a = __builtin_bit_cast(u32x2, __builtin_amdgcn_ds_read_tr16_b64_v4i16((LAS s16x4*)(L + off0)));
  u32x2 b = __builtin_bit_cast(u32x2, __builtin_amdgcn_ds_read_tr16_b64_v4i16((LAS s16x4*)(L + off1)));
  return u32x4{a.x, a.y, b.x, b.y};
}
template <int CTRL> __device__ __forceinline__ float dpp(float x) {
  return __builtin_bit_cast(float, __builtin_amdgcn_mov_dpp(__builtin_bit_cast(int, x), CTRL, 0xf, 0xf, true));
}
__device__ __forceinline__ float xrow_sum(float x) {
  auto s = __builtin_amdgcn_permlane16_swap(__float_as_uint(x), __float_as_uint(x), false, false);
  x = __uint_as_float(s[0]) + __uint_as_float(s[1]);
  auto t = __builtin_amdgcn_permlane32_swap(__float_as_uint(x), __float_as_uint(x), false, false);
  return __uint_as_float(t[0]) + __uint_as_float(t[1]);
}
__device__ __forceinline__ float xrow_max(float x) {
  auto s = __builtin_amdgcn_permlane16_swap(__float_as_uint(x), __float_as_uint(x), false, false);
  x = fmaxf(__uint_as_float(s[0]), __uint_as_float(s[1]));
  auto t = __builtin_amdgcn_permlane32_swap(__float_as_uint(x), __float_as_uint(x), false, false);
  return fmaxf(__uint_as_float(t[0]), __uint_as_float(t[1]));
}
__device__ __forceinline__ float wave_sum(float x) {
  x += dpp<0xB1>(x);
  x += dpp<0x4E>(x);
  x += dpp<0x141>(x);
  x += dpp<0x140>(x);
  return xrow_sum(x);
}
__device__ __forceinline__ u32x4 ldmb(__amdgpu_buffer_rsrc_t rs, u32 off) { return __builtin_amdgcn_raw_buffer_load_b128(rs, (int)off, 0, AUX_SC1); }
__device__ __forceinline__ void stmb(__amdgpu_buffer_rsrc_t rs, u32 off, u32x4 v) { __builtin_amdgcn_raw_buffer_store_b128(v, rs, (int)off, 0, AUX_SC1); }
__device__ __forceinline__ bool tagok(u32x4 v, u32 tA, u32 tB) { return v.y == tA && v.w == tB; }
__device__ __forceinline__ bool allok(bool ok) { return __builtin_amdgcn_ballot_w64(!ok) == 0ull; }

#define STAMP(k)
#define STAMP_DECL
#define STAMP_FLUSH()
}

__global__ void __launch_bounds__(768) fused_decode_kernel(const u16* xp_, const u16* kc_, const u16* vc_,
                                                           const u16* wq_, const u16* wo_, const u16* w1_,
                                                           const u16* w3_, const u16* w2_, u16* out_,
                                                           void* ws_) {
  typedef const __attribute__((address_space(4))) u64* kargp;
  kargp ka = (kargp)__builtin_amdgcn_kernarg_segment_ptr();
  const u16* xp = (const u16*)ka[0]; const u16* kc = (const u16*)ka[1]; const u16* vc = (const u16*)ka[2];
  const u16* wq = (const u16*)ka[3]; const u16* wo = (const u16*)ka[4]; const u16* w1 = (const u16*)ka[5];
  const u16* w3 = (const u16*)ka[6]; const u16* w2 = (const u16*)ka[7]; u16* out = (u16*)ka[8]; void* ws = (void*)ka[9];
  __shared__ __attribute__((aligned(16))) unsigned char smem[L_END];
  LAS unsigned char* L = (LAS unsigned char*)smem;
  const u32 lb = (u32)(uintptr_t)L;
  const int tid = threadIdx.x, lane = tid & 63;
  const int w = __builtin_amdgcn_readfirstlane(tid >> 6);
  const int c = blockIdx.x, x = c & 7, j = c >> 3;
  const bool ffn = j < 28;
  const int n16 = lane & 15, g = lane >> 4;
  const __amdgpu_buffer_rsrc_t rs = __builtin_amdgcn_make_buffer_rsrc(ws, 0, (int)WS_END, 0x00020000);
  STAMP_DECL;
  STAMP(0);

  if (w < 8) {
    const int b = j >> 3, s = j & 7;
    const size_t kvb = ((size_t)((b * 8 + x) * 4096 + 3072 + 128 * s + 16 * w)) * 256;
    const char* kb = (const char*)kc + kvb;
    const char* vb = (const char*)vc + kvb;
    const int lg = 7 * x + (j >> 2), mtx = lg & 1, G1 = lg >> 1;
    {
      const char* sb = (const char*)wq + (size_t)(64 * w) * 8192 + (512 * x + 16 * j) * 2;
      const u32 vo = (u32)(lane >> 1) * 8192 + (lane & 1) * 16;
      dma_w(sb, vo, lb + L_WQ + (64 * w) * 32);
      dma_w(sb + 32 * 8192, vo, lb + L_WQ + (64 * w + 32) * 32);
    }
    if (ffn) {
      const char* sb = (const char*)(mtx ? w3 : w1) + (size_t)(64 * w) * 3584 + (64 * G1 + 16 * (j & 3)) * 2;
      const u32 vo = (u32)(lane >> 1) * 3584 + (lane & 1) * 16;
      dma_w(sb, vo, lb + L_WG + (64 * w) * 32);
      dma_w(sb + 32 * 3584, vo, lb + L_WG + (64 * w + 32) * 32);
    }
#pragma unroll
    for (int i = 0; i < 4; ++i) {
      const u32 r = 4 * i + g;
      dma_kv(kb, r * 256 + ((n16 ^ r) * 16), lb + L_K + (16 * w + 4 * i) * 256);
    }
#pragma unroll
    for (int i = 0; i < 4; ++i) {
      const u32 r = 4 * i + g;
      dma_kv(vb, r * 256 + ((n16 ^ ((g << 2) | i)) * 16), lb + L_V + (16 * w + 4 * i) * 256);
    }
    {
      const int dt = j >> 2, cg4 = j & 3;
      const char* sb = (const char*)wo + (size_t)((4 * x + (w >> 1)) * 128 + 16 * dt + 8 * (w & 1)) * 1024 + cg4 * 256;
#pragma unroll
      for (int i = 0; i < 2; ++i)
        dma_w(sb, (u32)(4 * i + g) * 1024 + ((n16 ^ ((g << 2) | ((2 * w + i) & 3))) * 16), lb + L_WO + (8 * w + 4 * i) * 256);
    }
    if (ffn) {
      const char* sb = (const char*)w2 + (size_t)(64 * j + 8 * w) * 1024 + x * 128;
      dma_w(sb, (u32)(lane >> 3) * 1024 + ((((lane & 7) >> 1) ^ (g & 3)) * 32) + (lane & 1) * 16, lb + L_W2 + 8 * w * 128);
    }
    STAMP(1);
    BAR();
    STAMP(2);
    const u32 tA = *(LAS u32*)(L + L_TAG), tB = *(LAS u32*)(L + L_TAG + 4);
    {
      const u32 xo = L_XN + (n16 & 3) * XN_S + g * 16 + 2 * w * 64;
      const u32x4 xa = lds128(L, xo), xb = lds128(L, xo + 64);
      if (ffn) VMWAIT(13); else VMWAIT(10);
      f32x4 acc = {0.f, 0.f, 0.f, 0.f};
      const u32 o = L_WQ + (64 * w) * 32 + lane * 8;
      acc = mfma(tr2(L, o, o + 512), xa, acc);
      acc = mfma(tr2(L, o + 1024, o + 1536), xb, acc);
      if (n16 < 4) *(LAS f32x4*)(L + L_QP + w * 256 + n16 * 64 + g * 16) = acc;
      if (ffn) {
        VMWAIT(11);
        f32x4 a2 = {0.f, 0.f, 0.f, 0.f};
        const u32 o2 = L_WG + (64 * w) * 32 + lane * 8;
        a2 = mfma(tr2(L, o2, o2 + 512), xa, a2);
        a2 = mfma(tr2(L, o2 + 1024, o2 + 1536), xb, a2);
        if (n16 < 4) *(LAS f32x4*)(L + L_GP + w * 256 + n16 * 64 + g * 16) = a2;
      }
    }
    STAMP(3);
    BAR();
    if (ffn) VMWAIT(7); else VMWAIT(6);
    STAMP(4);
    u32x4 kf[4];
#pragma unroll
    for (int st = 0; st < 4; ++st) kf[st] = lds128(L, L_K + (16 * w + n16) * 256 + (((4 * st + g) ^ n16) * 16));
    BAR();
    STAMP(5);
    f32x4 sc = {0.f, 0.f, 0.f, 0.f};
#pragma unroll
    for (int st = 0; st < 4; ++st) sc = mfma(kf[st], lds128(L, L_QI + (n16 & 3) * QI_S + st * 64 + g * 16), sc);
    float mx = xrow_max(fmaxf(fmaxf(sc.x, sc.y), fmaxf(sc.z, sc.w)));
    if (lane < 4) *(LAS float*)(L + L_MX + lane * 32 + w * 4) = mx;
    BAR();
    float Mc;
    {
      const f32x4 m0 = *(LAS f32x4*)(L + L_MX + (n16 & 3) * 32), m1 = *(LAS f32x4*)(L + L_MX + (n16 & 3) * 32 + 16);
      Mc = fmaxf(fmaxf(fmaxf(m0.x, m0.y), fmaxf(m0.z, m0.w)), fmaxf(fmaxf(m1.x, m1.y), fmaxf(m1.z, m1.w))) * SM_C;
      const float p0 = __builtin_amdgcn_exp2f(fmaf(sc.x, SM_C, -Mc)), p1 = __builtin_amdgcn_exp2f(fmaf(sc.y, SM_C, -Mc));
      const float p2 = __builtin_amdgcn_exp2f(fmaf(sc.z, SM_C, -Mc)), p3 = __builtin_amdgcn_exp2f(fmaf(sc.w, SM_C, -Mc));
      if (n16 < 4) *(LAS u32x2*)(L + L_PI + n16 * QI_S + (16 * w + 4 * g) * 2) = u32x2{pk(p0, p1), pk(p2, p3)};
    }
    if (ffn) VMWAIT(3); else VMWAIT(2);
    STAMP(6);
    BAR();
    {
      const int q4 = n16 >> 2, p4 = lane & 3;
      f32x4 o = {0.f, 0.f, 0.f, 0.f}, l = {0.f, 0.f, 0.f, 0.f};
      const u32x4 ones = {0x3F803F80u, 0x3F803F80u, 0x3F803F80u, 0x3F803F80u};
#pragma unroll
      for (int st = 0; st < 4; ++st) {
        const u32x4 pf = lds128(L, L_PI + (n16 & 3) * QI_S + st * 64 + g * 16);
        const u32 r0 = 32 * st + 8 * g + q4, r1 = r0 + 4;
        const u32 ch = 2 * w + (p4 >> 1);
        const u32 a0 = L_V + r0 * 256 + ((ch ^ ((q4 << 2) | ((2 * g) & 3))) * 16) + 8 * (p4 & 1);
        const u32 a1 = L_V + r1 * 256 + ((ch ^ ((q4 << 2) | ((2 * g + 1) & 3))) * 16) + 8 * (p4 & 1);
        o = mfma(pf, tr2(L, a0, a1), o);
        l = mfma(pf, ones, l);
      }
      if (lane < 16) stmb(rs, WS_O + (((x * 8 + w) * 32 + j) * 16 + lane) * 16, u32x4{pk(o.x, o.y), tA, pk(o.z, o.w), tB});
      if (w == 0 && lane < 4) {
        const float lv = lane == 0 ? l.x : lane == 1 ? l.y : lane == 2 ? l.z : l.w;
        stmb(rs, WS_S + ((x * 32 + j) * 4 + lane) * 16, u32x4{__float_as_uint(Mc), tA, __float_as_uint(lv), tB});
      }
    }
    STAMP(7);
    if (w == 0) VMWAIT(2); else VMWAIT(1);
    STAMP(8);
    BAR();
    STAMP(9);
    {
      const int q4 = n16 >> 2, p4 = lane & 3;
      f32x4 a = {0.f, 0.f, 0.f, 0.f};
#pragma unroll
      for (int st = 0; st < 2; ++st) {
        const u32x4 of = lds128(L, L_OA + (n16 & 3) * OA_S + st * 64 + g * 16);
        const u32 r0 = 32 * st + 8 * g + q4, r1 = r0 + 4;
        const u32 ch = 2 * w + (p4 >> 1);
        const u32 a0 = L_WO + r0 * 256 + ((ch ^ ((q4 << 2) | ((2 * g) & 3))) * 16) + 8 * (p4 & 1);
        const u32 a1 = L_WO + r1 * 256 + ((ch ^ ((q4 << 2) | ((2 * g + 1) & 3))) * 16) + 8 * (p4 & 1);
        a = mfma(of, tr2(L, a0, a1), a);
      }
      if (lane < 16) {
        const int cq = 32 * (j & 3) + 4 * w + (lane >> 2), slot = x * 8 + (j >> 2);
        const u32 o0 = WS_H + (((cq * 2) * 96 + slot) * 4 + (lane & 3)) * 16;
        stmb(rs, o0, u32x4{__float_as_uint(a.x), tA, __float_as_uint(a.y), tB});
        stmb(rs, o0 + 96 * 64, u32x4{__float_as_uint(a.z), tA, __float_as_uint(a.w), tB});
      }
      if (ffn && w < 4) {
        f32x4 f = {0.f, 0.f, 0.f, 0.f};
#pragma unroll
        for (int st = 0; st < 2; ++st) {
          const u32x4 hf = lds128(L, L_HA + (n16 & 3) * OA_S + st * 64 + g * 16);
          const u32 r0 = 32 * st + 4 * g + q4, r1 = r0 + 16;
          const u32 ph = (u32)(w ^ ((2 * g + (q4 >> 1)) & 3));
          f = mfma(hf, tr2(L, L_W2 + r0 * 128 + ph * 32 + 8 * p4, L_W2 + r1 * 128 + ph * 32 + 8 * p4), f);
        }
        if (lane < 16) {
          const int cq = 16 * x + 4 * w + (lane >> 2), slot = 64 + j;
          const u32 o0 = WS_H + (((cq * 2) * 96 + slot) * 4 + (lane & 3)) * 16;
          stmb(rs, o0, u32x4{__float_as_uint(f.x), tA, __float_as_uint(f.y), tB});
          stmb(rs, o0 + 96 * 64, u32x4{__float_as_uint(f.z), tA, __float_as_uint(f.w), tB});
        }
      }
    }
    STAMP(10);
    STAMP_FLUSH();
  } else {
    const int cb = w - 8;
    u64 E = 0;
    if (cb == 0) E = __hip_atomic_load((GAS u64*)ws, __ATOMIC_RELAXED, __HIP_MEMORY_SCOPE_AGENT);
    const u32x4 xv = *(const GAS u32x4*)(xp + cb * 512 + lane * 8);
    const int cq = c >> 1, bp = c & 1;
    u32 xr0 = 0, xr1 = 0;
    if (cb == 0) {
      xr0 = ((const GAS u16*)xp)[(2 * bp) * 512 + 4 * cq + (lane & 3)];
      xr1 = ((const GAS u16*)xp)[(2 * bp + 1) * 512 + 4 * cq + (lane & 3)];
    }
    {
      float v[8] = {bflo(xv.x), bfhi(xv.x), bflo(xv.y), bfhi(xv.y), bflo(xv.z), bfhi(xv.z), bflo(xv.w), bfhi(xv.w)};
      float sm = ((v[0] + v[1]) + (v[2] + v[3])) + ((v[4] + v[5]) + (v[6] + v[7]));
      const float mean = wave_sum(sm) * (1.0f / 512.0f);
      float sq = 0.f;
#pragma unroll
      for (int e = 0; e < 8; ++e) { v[e] -= mean; sq = fmaf(v[e], v[e], sq); }
      const float rstd = 1.0f / sqrtf(wave_sum(sq) * (1.0f / 512.0f) + 1e-5f);
      const u32 o = L_XN + cb * XN_S + (lane >> 2) * 64 + (lane & 1) * 32 + ((lane & 3) >> 1) * 8;
      *(LAS u32x2*)(L + o) = u32x2{pk(v[0] * rstd, v[1] * rstd), pk(v[2] * rstd, v[3] * rstd)};
      *(LAS u32x2*)(L + o + 16) = u32x2{pk(v[4] * rstd, v[5] * rstd), pk(v[6] * rstd, v[7] * rstd)};
    }
    u32 tA, tB;
    u64 T = 0;
    if (cb == 0) {
      T = E + 1;
      tA = (u32)T;
      tB = (u32)(E >> 32) ^ ((tA << 1) | 1u);
      if (lane == 0) *(LAS u32x2*)(L + L_TAG) = u32x2{tA, tB};
    }
    STAMP(1);
    BAR();
    STAMP(2);
    tA = *(LAS u32*)(L + L_TAG);
    tB = *(LAS u32*)(L + L_TAG + 4);
    BAR();
    STAMP(3);
    if (cb == 0) {
      f32x4 a = *(LAS f32x4*)(L + L_QP + lane * 16), a2 = *(LAS f32x4*)(L + L_QP + 1024 + lane * 16);
      a += a2;
      a.x = xrow_sum(a.x); a.y = xrow_sum(a.y); a.z = xrow_sum(a.z); a.w = xrow_sum(a.w);
      if (lane < 16) stmb(rs, WS_Q + ((x * 4 + (lane >> 2)) * 128 + 4 * j + (lane & 3)) * 16, u32x4{pk(a.x, a.y), tA, pk(a.z, a.w), tB});
      STAMP(4);
      const u32 qo = WS_Q + ((x * 4 + (j >> 3)) * 128 + lane) * 16;
      u32x4 g0, g1;
      for (int it = 0; it < SPIN_CAP; ++it) {
        asm volatile("" ::: "memory");
        g0 = ldmb(rs, qo);
        g1 = ldmb(rs, qo + 1024);
        if (allok(tagok(g0, tA, tB) && tagok(g1, tA, tB))) break;
      }
      *(LAS u32x2*)(L + L_QI + (lane >> 5) * QI_S + (lane & 31) * 8) = u32x2{g0.x, g0.z};
      *(LAS u32x2*)(L + L_QI + (2 + (lane >> 5)) * QI_S + (lane & 31) * 8) = u32x2{g1.x, g1.z};
      STAMP(5);
    } else if (cb == 1 && ffn) {
      f32x4 a = *(LAS f32x4*)(L + L_GP + lane * 16), a2 = *(LAS f32x4*)(L + L_GP + 1024 + lane * 16);
      a += a2;
      a.x = xrow_sum(a.x); a.y = xrow_sum(a.y); a.z = xrow_sum(a.z); a.w = xrow_sum(a.w);
      const int lg = 7 * x + (j >> 2);
      if (lane < 16) stmb(rs, WS_G + ((lg * 4 + (lane >> 2)) * 16 + 4 * (j & 3) + (lane & 3)) * 16, u32x4{pk(a.x, a.y), tA, pk(a.z, a.w), tB});
    }
    BAR();
    BAR();
    BAR();
    STAMP(6);
    {
      const u32 so = WS_S + ((x * 32 + 8 * cb + 2 * g) * 4 + (n16 & 3)) * 16;
      const u32 oo = WS_O + (((x * 8 + (j >> 2)) * 32 + 8 * cb + 2 * g) * 16 + n16) * 16;
      u32x4 sa, sb, da, db;
      if (cb == 1 && ffn) {
        const u32 go = WS_G + ((2 * j) * 64 + lane) * 16;
        u32x4 gg, uu;
        for (int it = 0; it < SPIN_CAP; ++it) {
          asm volatile("" ::: "memory");
          gg = ldmb(rs, go);
          uu = ldmb(rs, go + 1024);
          if (allok(tagok(gg, tA, tB) && tagok(uu, tA, tB))) break;
        }
        const float g0 = bflo(gg.x), g1 = bfhi(gg.x), g2 = bflo(gg.z), g3 = bfhi(gg.z);
        const float h0 = g0 * __builtin_amdgcn_rcpf(1.f + __builtin_amdgcn_exp2f(-g0 * LOG2E)) * bflo(uu.x);
        const float h1 = g1 * __builtin_amdgcn_rcpf(1.f + __builtin_amdgcn_exp2f(-g1 * LOG2E)) * bfhi(uu.x);
        const float h2 = g2 * __builtin_amdgcn_rcpf(1.f + __builtin_amdgcn_exp2f(-g2 * LOG2E)) * bflo(uu.z);
        const float h3 = g3 * __builtin_amdgcn_rcpf(1.f + __builtin_amdgcn_exp2f(-g3 * LOG2E)) * bfhi(uu.z);
        *(LAS u32x2*)(L + L_HA + g * OA_S + (n16 >> 3) * 64 + (n16 & 3) * 16 + ((n16 & 7) >> 2) * 8) = u32x2{pk(h0, h1), pk(h2, h3)};
      }
      for (int it = 0; it < SPIN_CAP; ++it) {
        asm volatile("" ::: "memory");
        sa = ldmb(rs, so); sb = ldmb(rs, so + 64);
        da = ldmb(rs, oo); db = ldmb(rs, oo + 256);
        if (allok(tagok(sa, tA, tB) && tagok(sb, tA, tB) && tagok(da, tA, tB) && tagok(db, tA, tB))) break;
      }
      STAMP(7);
      const float Ma = __uint_as_float(sa.x), La = __uint_as_float(sa.z), Mb = __uint_as_float(sb.x), Lb = __uint_as_float(sb.z);
      const float Mx = xrow_max(fmaxf(Ma, Mb));
      const float ea = __builtin_amdgcn_exp2f(Ma - Mx), eb = __builtin_amdgcn_exp2f(Mb - Mx);
      const float r = 1.0f / xrow_sum(fmaf(ea, La, eb * Lb));
      const u32 wp = pk(ea * r, eb * r);
      const u32 sh = (n16 & 1) * 16;
      const u32 A_ = (wp & 0xffffu) << sh, B_ = (wp >> 16) << sh;
      const bool lo = (n16 & 2) == 0;
      const u32x4 wf = {lo ? A_ : 0u, lo ? 0u : A_, lo ? B_ : 0u, lo ? 0u : B_};
      f32x4 d = {0.f, 0.f, 0.f, 0.f};
      d = mfma(u32x4{da.x, da.z, db.x, db.z}, wf, d);
      if (n16 < 4) *(LAS u32x2*)(L + L_OA + cb * OA_S + (16 * n16 + 4 * g) * 2) = u32x2{pk(d.x, d.y), pk(d.z, d.w)};
    }
    STAMP(8);
    BAR();
    if (cb == 0) {
      const u32 ho = WS_H + (c * 96 * 4 + lane) * 16;
      u32x4 h[6];
      const bool v5 = lane < 48;
      for (int it = 0; it < SPIN_CAP; ++it) {
        asm volatile("" ::: "memory");
        bool ok = true;
#pragma unroll
        for (int i = 0; i < 5; ++i) h[i] = ldmb(rs, ho + i * 1024);
        h[5] = ldmb(rs, ho + (v5 ? 5 : 4) * 1024);
#pragma unroll
        for (int i = 0; i < 6; ++i) ok = ok && tagok(h[i], tA, tB);
        if (allok(ok)) break;
      }
      STAMP(9);
      float s0 = 0.f, s1 = 0.f;
#pragma unroll
      for (int i = 0; i < 4; ++i) { s0 += __uint_as_float(h[i].x); s1 += __uint_as_float(h[i].z); }
      float f0 = __uint_as_float(h[4].x) + (v5 ? __uint_as_float(h[5].x) : 0.f);
      float f1 = __uint_as_float(h[4].z) + (v5 ? __uint_as_float(h[5].z) : 0.f);
      s0 += dpp<0x124>(s0); s1 += dpp<0x124>(s1); f0 += dpp<0x124>(f0); f1 += dpp<0x124>(f1);
      s0 += dpp<0x128>(s0); s1 += dpp<0x128>(s1); f0 += dpp<0x128>(f0); f1 += dpp<0x128>(f1);
      s0 = xrow_sum(s0); s1 = xrow_sum(s1); f0 = xrow_sum(f0); f1 = xrow_sum(f1);
      const float y0 = bflo(xr0) + s0 * (f0 * __builtin_amdgcn_rcpf(1.f + __builtin_amdgcn_exp2f(-f0 * LOG2E)));
      const float y1 = bflo(xr1) + s1 * (f1 * __builtin_amdgcn_rcpf(1.f + __builtin_amdgcn_exp2f(-f1 * LOG2E)));
      if (lane < 4) {
        const u32 y = pk(y0, y1);
        ((GAS u16*)out)[(2 * bp) * 512 + 4 * cq + lane] = (u16)y;
        ((GAS u16*)out)[(2 * bp + 1) * 512 + 4 * cq + lane] = (u16)(y >> 16);
      }
      if (c == 0 && lane == 0) __hip_atomic_store((GAS u64*)ws, T, __ATOMIC_RELAXED, __HIP_MEMORY_SCOPE_AGENT);
      STAMP(10);
    }
    STAMP_FLUSH();
  }
}

extern "C" void kernel_launch(void* const* d_in, const int* in_sizes, int n_in, void* d_out, int out_size, void* d_ws, size_t ws_size,
                              hipStream_t stream) {
  fused_decode_kernel<<<256, 768, 0, stream>>>((const u16*)d_in[0], (const u16*)d_in[1], (const u16*)d_in[2], (const u16*)d_in[3],
                                               (const u16*)d_in[4], (const u16*)d_in[6], (const u16*)d_in[7], (const u16*)d_in[8],
                                               (u16*)d_out, d_ws);
}
```

```cpp
#include <hip/hip_runtime.h>
#include <stdint.h>

namespace {
typedef unsigned short u16;
typedef unsigned u32;
typedef unsigned long long u64;
typedef u32 u32x4 __attribute__((ext_vector_type(4)));
typedef u32 u32x2 __attribute__((ext_vector_type(2)));
typedef float f32x4 __attribute__((ext_vector_type(4)));
typedef float f32x2 __attribute__((ext_vector_type(2)));
typedef short s16x4 __attribute__((ext_vector_type(4)));
typedef __bf16 bf16x2 __attribute__((ext_vector_type(2)));
typedef __bf16 bf16x8 __attribute__((ext_vector_type(8)));
#define LAS __attribute__((address_space(3)))
#define GAS __attribute__((address_space(1)))

constexpr u32 WS_EPOCH = 0;
constexpr u32 WS_Q = 4096;
constexpr u32 WS_G = WS_Q + 65536;
constexpr u32 WS_S = WS_G + 57344;
constexpr u32 WS_O = WS_S + 16384;
constexpr u32 WS_H = WS_O + 524288;
constexpr u32 WS_END = WS_H + 256 * 96 * 64;
constexpr u32 WS_DBG = (WS_END + 4095) & ~4095u;

constexpr u32 L_WQ = 0, L_WG = 16384, L_K = 32768, L_V = 65536, L_WO = 98304, L_W2 = 114688;
constexpr u32 L_XN = 122880, XN_S = 1088;
constexpr u32 L_QP = L_XN + 4 * XN_S;
constexpr u32 L_GP = L_QP + 2048;
constexpr u32 L_QI = L_GP + 2048, QI_S = 320;
constexpr u32 L_PI = L_QI + 4 * QI_S;
constexpr u32 L_MX = L_PI + 4 * QI_S;
constexpr u32 L_OA = L_MX + 128, OA_S = 192;
constexpr u32 L_HA = L_OA + 4 * OA_S;
constexpr u32 L_TAG = L_HA + 4 * OA_S;
constexpr u32 L_END = L_TAG + 16;

constexpr float SM_C = 0.08838834764831845f * 1.4426950408889634f;
constexpr float LOG2E = 1.4426950408889634f;
constexpr int SPIN_CAP = 1 << 16;
constexpr int AUX_SC1 = 16;

#define KV_POL 1
#define W_POL 1
#define POLSTR_0 ""
#define POLSTR_1 " nt"
#define POLSTR_2 " sc1"
#define POLSTR_3 " sc0 sc1 nt"
#define POLSTR_4 " sc1 nt"
#define POLSTR_5 " sc0 sc1"
#define POLCAT(x) POLSTR_##x
#define POLSTR(x) POLCAT(x)
#define KV_NT POLSTR(KV_POL)
#define W_NT POLSTR(W_POL)

__device__ __forceinline__ void dma_kv(const void* sbase, u32 voff, u32 lds) {
  asm volatile("s_mov_b32 m0, %2\n\ts_nop 4\n\tglobal_load_lds_dwordx4 %0, %1" KV_NT ::"v"(voff), "s"(sbase), "s"(lds) : "memory");
}
__device__ __forceinline__ void dma_w(const void* sbase, u32 voff, u32 lds) {
  asm volatile("s_mov_b32 m0, %2\n\ts_nop 4\n\tglobal_load_lds_dwordx4 %0, %1" W_NT ::"v"(voff), "s"(sbase), "s"(lds) : "memory");
}
#define VMWAIT(n) asm volatile("s_waitcnt vmcnt(" #n ")" ::: "memory")
#define BAR() asm volatile("s_waitcnt lgkmcnt(0)\n\ts_barrier" ::: "memory")

__device__ __forceinline__ u32 pk(float lo, float hi) {
  f32x2 v = {lo, hi};
  return __builtin_bit_cast(u32, __builtin_convertvector(v, bf16x2));
}
__device__ __forceinline__ float bflo(u32 v) { return __uint_as_float(v << 16); }
__device__ __forceinline__ float bfhi(u32 v) { return __uint_as_float(v & 0xffff0000u); }
__device__ __forceinline__ f32x4 mfma(u32x4 a, u32x4 b, f32x4 c) {
  return __builtin_amdgcn_mfma_f32_16x16x32_bf16(__builtin_bit_cast(bf16x8, a), __builtin_bit_cast(bf16x8, b), c, 0, 0, 0);
}
__device__ __forceinline__ u32x4 lds128(LAS unsigned char* L, u32 off) { return *(LAS u32x4*)(L + off); }
__device__ __forceinline__ u32x4 tr2(LAS unsigned char* L, u32 off0, u32 off1) {
  u32x2 a = __builtin_bit_cast(u32x2, __builtin_amdgcn_ds_read_tr16_b64_v4i16((LAS s16x4*)(L + off0)));
  u32x2 b = __builtin_bit_cast(u32x2, __builtin_amdgcn_ds_read_tr16_b64_v4i16((LAS s16x4*)(L + off1)));
  return u32x4{a.x, a.y, b.x, b.y};
}
template <int CTRL> __device__ __forceinline__ float dpp(float x) {
  return __builtin_bit_cast(float, __builtin_amdgcn_mov_dpp(__builtin_bit_cast(int, x), CTRL, 0xf, 0xf, true));
}
__device__ __forceinline__ float xrow_sum(float x) {
  auto s = __builtin_amdgcn_permlane16_swap(__float_as_uint(x), __float_as_uint(x), false, false);
  x = __uint_as_float(s[0]) + __uint_as_float(s[1]);
  auto t = __builtin_amdgcn_permlane32_swap(__float_as_uint(x), __float_as_uint(x), false, false);
  return __uint_as_float(t[0]) + __uint_as_float(t[1]);
}
__device__ __forceinline__ float xrow_max(float x) {
  auto s = __builtin_amdgcn_permlane16_swap(__float_as_uint(x), __float_as_uint(x), false, false);
  x = fmaxf(__uint_as_float(s[0]), __uint_as_float(s[1]));
  auto t = __builtin_amdgcn_permlane32_swap(__float_as_uint(x), __float_as_uint(x), false, false);
  return fmaxf(__uint_as_float(t[0]), __uint_as_float(t[1]));
}
__device__ __forceinline__ float wave_sum(float x) {
  x += dpp<0xB1>(x);
  x += dpp<0x4E>(x);
  x += dpp<0x141>(x);
  x += dpp<0x140>(x);
  return xrow_sum(x);
}
__device__ __forceinline__ u32x4 ldmb(__amdgpu_buffer_rsrc_t rs, u32 off) { return __builtin_amdgcn_raw_buffer_load_b128(rs, (int)off, 0, AUX_SC1); }
__device__ __forceinline__ void stmb(__amdgpu_buffer_rsrc_t rs, u32 off, u32x4 v) { __builtin_amdgcn_raw_buffer_store_b128(v, rs, (int)off, 0, AUX_SC1); }
__device__ __forceinline__ bool tagok(u32x4 v, u32 tA, u32 tB) { return v.y == tA && v.w == tB; }
__device__ __forceinline__ bool allok(bool ok) { return __builtin_amdgcn_ballot_w64(!ok) == 0ull; }

#define STAMP(k)
#define STAMP_DECL
#define STAMP_FLUSH()
}

__global__ void __launch_bounds__(1024) fused_decode_kernel(const u16* xp_, const u16* kc_, const u16* vc_,
                                                           const u16* wq_, const u16* wo_, const u16* w1_,
                                                           const u16* w3_, const u16* w2_, u16* out_,
                                                           void* ws_) {
  typedef const __attribute__((address_space(4))) u64* kargp;
  kargp ka = (kargp)__builtin_amdgcn_kernarg_segment_ptr();
  const u16* xp = (const u16*)ka[0]; const u16* kc = (const u16*)ka[1]; const u16* vc = (const u16*)ka[2];
  const u16* wq = (const u16*)ka[3]; const u16* wo = (const u16*)ka[4]; const u16* w1 = (const u16*)ka[5];
  const u16* w3 = (const u16*)ka[6]; const u16* w2 = (const u16*)ka[7]; u16* out = (u16*)ka[8]; void* ws = (void*)ka[9];
  __shared__ __attribute__((aligned(16))) unsigned char smem[L_END];
  LAS unsigned char* L = (LAS unsigned char*)smem;
  const u32 lb = (u32)(uintptr_t)L;
  const int tid = threadIdx.x, lane = tid & 63;
  const int w = __builtin_amdgcn_readfirstlane(tid >> 6);
  const int c = blockIdx.x, x = c & 7, j = c >> 3;
  const bool ffn = j < 28;
  const int n16 = lane & 15, g = lane >> 4;
  const __amdgpu_buffer_rsrc_t rs = __builtin_amdgcn_make_buffer_rsrc(ws, 0, (int)WS_END, 0x00020000);
  STAMP_DECL;
  STAMP(0);

  if (w >= 12) return;
  if (w < 8) {
    const int b = j >> 3, s = j & 7;
    const size_t kvb = ((size_t)((b * 8 + x) * 4096 + 3072 + 128 * s + 16 * w)) * 256;
    const char* kb = (const char*)kc + kvb;
    const char* vb = (const char*)vc + kvb;
    const int lg = 7 * x + (j >> 2), mtx = lg & 1, G1 = lg >> 1;
    {
      const char* sb = (const char*)wq + (size_t)(64 * w) * 8192 + (512 * x + 16 * j) * 2;
      const u32 vo = (u32)(lane >> 1) * 8192 + (lane & 1) * 16;
      dma_w(sb, vo, lb + L_WQ + (64 * w) * 32);
      dma_w(sb + 32 * 8192, vo, lb + L_WQ + (64 * w + 32) * 32);
    }
    if (ffn) {
      const char* sb = (const char*)(mtx ? w3 : w1) + (size_t)(64 * w) * 3584 + (64 * G1 + 16 * (j & 3)) * 2;
      const u32 vo = (u32)(lane >> 1) * 3584 + (lane & 1) * 16;
      dma_w(sb, vo, lb + L_WG + (64 * w) * 32);
      dma_w(sb + 32 * 3584, vo, lb + L_WG + (64 * w + 32) * 32);
    }
#pragma unroll
    for (int i = 0; i < 4; ++i) {
      const u32 r = 4 * i + g;
      dma_kv(kb, r * 256 + ((n16 ^ r) * 16), lb + L_K + (16 * w + 4 * i) * 256);
    }
#pragma unroll
    for (int i = 0; i < 4; ++i) {
      const u32 r = 4 * i + g;
      dma_kv(vb, r * 256 + ((n16 ^ ((g << 2) | i)) * 16), lb + L_V + (16 * w + 4 * i) * 256);
    }
    {
      const int dt = j >> 2, cg4 = j & 3;
      const char* sb = (const char*)wo + (size_t)((4 * x + (w >> 1)) * 128 + 16 * dt + 8 * (w & 1)) * 1024 + cg4 * 256;
#pragma unroll
      for (int i = 0; i < 2; ++i)
        dma_w(sb, (u32)(4 * i + g) * 1024 + ((n16 ^ ((g << 2) | ((2 * w + i) & 3))) * 16), lb + L_WO + (8 * w + 4 * i) * 256);
    }
    if (ffn) {
      const char* sb = (const char*)w2 + (size_t)(64 * j + 8 * w) * 1024 + x * 128;
      dma_w(sb, (u32)(lane >> 3) * 1024 + ((((lane & 7) >> 1) ^ (g & 3)) * 32) + (lane & 1) * 16, lb + L_W2 + 8 * w * 128);
    }
    STAMP(1);
    BAR();
    STAMP(2);
    const u32 tA = *(LAS u32*)(L + L_TAG), tB = *(LAS u32*)(L + L_TAG + 4);
    {
      const u32 xo = L_XN + (n16 & 3) * XN_S + g * 16 + 2 * w * 64;
      const u32x4 xa = lds128(L, xo), xb = lds128(L, xo + 64);
      if (ffn) VMWAIT(13); else VMWAIT(10);
      f32x4 acc = {0.f, 0.f, 0.f, 0.f};
      const u32 o = L_WQ + (64 * w) * 32 + lane * 8;
      acc = mfma(tr2(L, o, o + 512), xa, acc);
      acc = mfma(tr2(L, o + 1024, o + 1536), xb, acc);
      if (n16 < 4) *(LAS f32x4*)(L + L_QP + w * 256 + n16 * 64 + g * 16) = acc;
      if (ffn) {
        VMWAIT(11);
        f32x4 a2 = {0.f, 0.f, 0.f, 0.f};
        const u32 o2 = L_WG + (64 * w) * 32 + lane * 8;
        a2 = mfma(tr2(L, o2, o2 + 512), xa, a2);
        a2 = mfma(tr2(L, o2 + 1024, o2 + 1536), xb, a2);
        if (n16 < 4) *(LAS f32x4*)(L + L_GP + w * 256 + n16 * 64 + g * 16) = a2;
      }
    }
    STAMP(3);
    BAR();
    if (ffn) VMWAIT(7); else VMWAIT(6);
    STAMP(4);
    u32x4 kf[4];
#pragma unroll
    for (int st = 0; st < 4; ++st) kf[st] = lds128(L, L_K + (16 * w + n16) * 256 + (((4 * st + g) ^ n16) * 16));
    BAR();
    STAMP(5);
    f32x4 sc = {0.f, 0.f, 0.f, 0.f};
#pragma unroll
    for (int st = 0; st < 4; ++st) sc = mfma(kf[st], lds128(L, L_QI + (n16 & 3) * QI_S + st * 64 + g * 16), sc);
    float mx = xrow_max(fmaxf(fmaxf(sc.x, sc.y), fmaxf(sc.z, sc.w)));
    if (lane < 4) *(LAS float*)(L + L_MX + lane * 32 + w * 4) = mx;
    BAR();
    float Mc;
    {
      const f32x4 m0 = *(LAS f32x4*)(L + L_MX + (n16 & 3) * 32), m1 = *(LAS f32x4*)(L + L_MX + (n16 & 3) * 32 + 16);
      Mc = fmaxf(fmaxf(fmaxf(m0.x, m0.y), fmaxf(m0.z, m0.w)), fmaxf(fmaxf(m1.x, m1.y), fmaxf(m1.z, m1.w))) * SM_C;
      const float p0 = __builtin_amdgcn_exp2f(fmaf(sc.x, SM_C, -Mc)), p1 = __builtin_amdgcn_exp2f(fmaf(sc.y, SM_C, -Mc));
      const float p2 = __builtin_amdgcn_exp2f(fmaf(sc.z, SM_C, -Mc)), p3 = __builtin_amdgcn_exp2f(fmaf(sc.w, SM_C, -Mc));
      if (n16 < 4) *(LAS u32x2*)(L + L_PI + n16 * QI_S + (16 * w + 4 * g) * 2) = u32x2{pk(p0, p1), pk(p2, p3)};
    }
    if (ffn) VMWAIT(3); else VMWAIT(2);
    STAMP(6);
    BAR();
    {
      const int q4 = n16 >> 2, p4 = lane & 3;
      f32x4 o = {0.f, 0.f, 0.f, 0.f}, l = {0.f, 0.f, 0.f, 0.f};
      const u32x4 ones = {0x3F803F80u, 0x3F803F80u, 0x3F803F80u, 0x3F803F80u};
#pragma unroll
      for (int st = 0; st < 4; ++st) {
        const u32x4 pf = lds128(L, L_PI + (n16 & 3) * QI_S + st * 64 + g * 16);
        const u32 r0 = 32 * st + 8 * g + q4, r1 = r0 + 4;
        const u32 ch = 2 * w + (p4 >> 1);
        const u32 a0 = L_V + r0 * 256 + ((ch ^ ((q4 << 2) | ((2 * g) & 3))) * 16) + 8 * (p4 & 1);
        const u32 a1 = L_V + r1 * 256 + ((ch ^ ((q4 << 2) | ((2 * g + 1) & 3))) * 16) + 8 * (p4 & 1);
        o = mfma(pf, tr2(L, a0, a1), o);
        l = mfma(pf, ones, l);
      }
      if (lane < 16) stmb(rs, WS_O + (((x * 8 + w) * 32 + j) * 16 + lane) * 16, u32x4{pk(o.x, o.y), tA, pk(o.z, o.w), tB});
      if (w == 0 && lane < 4) {
        const float lv = lane == 0 ? l.x : lane == 1 ? l.y : lane == 2 ? l.z : l.w;
        stmb(rs, WS_S + ((x * 32 + j) * 4 + lane) * 16, u32x4{__float_as_uint(Mc), tA, __float_as_uint(lv), tB});
      }
    }
    STAMP(7);
    if (w == 0) VMWAIT(2); else VMWAIT(1);
    STAMP(8);
    BAR();
    STAMP(9);
    {
      const int q4 = n16 >> 2, p4 = lane & 3;
      f32x4 a = {0.f, 0.f, 0.f, 0.f};
#pragma unroll
      for (int st = 0; st < 2; ++st) {
        const u32x4 of = lds128(L, L_OA + (n16 & 3) * OA_S + st * 64 + g * 16);
        const u32 r0 = 32 * st + 8 * g + q4, r1 = r0 + 4;
        const u32 ch = 2 * w + (p4 >> 1);
        const u32 a0 = L_WO + r0 * 256 + ((ch ^ ((q4 << 2) | ((2 * g) & 3))) * 16) + 8 * (p4 & 1);
        const u32 a1 = L_WO + r1 * 256 + ((ch ^ ((q4 << 2) | ((2 * g + 1) & 3))) * 16) + 8 * (p4 & 1);
        a = mfma(of, tr2(L, a0, a1), a);
      }
      if (lane < 16) {
        const int cq = 32 * (j & 3) + 4 * w + (lane >> 2), slot = x * 8 + (j >> 2);
        const u32 o0 = WS_H + (((cq * 2) * 96 + slot) * 4 + (lane & 3)) * 16;
        stmb(rs, o0, u32x4{__float_as_uint(a.x), tA, __float_as_uint(a.y), tB});
        stmb(rs, o0 + 96 * 64, u32x4{__float_as_uint(a.z), tA, __float_as_uint(a.w), tB});
      }
      if (ffn && w < 4) {
        f32x4 f = {0.f, 0.f, 0.f, 0.f};
#pragma unroll
        for (int st = 0; st < 2; ++st) {
          const u32x4 hf = lds128(L, L_HA + (n16 & 3) * OA_S + st * 64 + g * 16);
          const u32 r0 = 32 * st + 4 * g + q4, r1 = r0 + 16;
          const u32 ph = (u32)(w ^ ((2 * g + (q4 >> 1)) & 3));
          f = mfma(hf, tr2(L, L_W2 + r0 * 128 + ph * 32 + 8 * p4, L_W2 + r1 * 128 + ph * 32 + 8 * p4), f);
        }
        if (lane < 16) {
          const int cq = 16 * x + 4 * w + (lane >> 2), slot = 64 + j;
          const u32 o0 = WS_H + (((cq * 2) * 96 + slot) * 4 + (lane & 3)) * 16;
          stmb(rs, o0, u32x4{__float_as_uint(f.x), tA, __float_as_uint(f.y), tB});
          stmb(rs, o0 + 96 * 64, u32x4{__float_as_uint(f.z), tA, __float_as_uint(f.w), tB});
        }
      }
    }
    STAMP(10);
    STAMP_FLUSH();
  } else {
    const int cb = w - 8;
    u64 E = 0;
    if (cb == 0) E = __hip_atomic_load((GAS u64*)ws, __ATOMIC_RELAXED, __HIP_MEMORY_SCOPE_AGENT);
    const u32x4 xv = *(const GAS u32x4*)(xp + cb * 512 + lane * 8);
    const int cq = c >> 1, bp = c & 1;
    u32 xr0 = 0, xr1 = 0;
    if (cb == 0) {
      xr0 = ((const GAS u16*)xp)[(2 * bp) * 512 + 4 * cq + (lane & 3)];
      xr1 = ((const GAS u16*)xp)[(2 * bp + 1) * 512 + 4 * cq + (lane & 3)];
    }
    {
      float v[8] = {bflo(xv.x), bfhi(xv.x), bflo(xv.y), bfhi(xv.y), bflo(xv.z), bfhi(xv.z), bflo(xv.w), bfhi(xv.w)};
      float sm = ((v[0] + v[1]) + (v[2] + v[3])) + ((v[4] + v[5]) + (v[6] + v[7]));
      const float mean = wave_sum(sm) * (1.0f / 512.0f);
      float sq = 0.f;
#pragma unroll
      for (int e = 0; e < 8; ++e) { v[e] -= mean; sq = fmaf(v[e], v[e], sq); }
      const float rstd = 1.0f / sqrtf(wave_sum(sq) * (1.0f / 512.0f) + 1e-5f);
      const u32 o = L_XN + cb * XN_S + (lane >> 2) * 64 + (lane & 1) * 32 + ((lane & 3) >> 1) * 8;
      *(LAS u32x2*)(L + o) = u32x2{pk(v[0] * rstd, v[1] * rstd), pk(v[2] * rstd, v[3] * rstd)};
      *(LAS u32x2*)(L + o + 16) = u32x2{pk(v[4] * rstd, v[5] * rstd), pk(v[6] * rstd, v[7] * rstd)};
    }
    u32 tA, tB;
    u64 T = 0;
    if (cb == 0) {
      T = E + 1;
      tA = (u32)T;
      tB = (u32)(E >> 32) ^ ((tA << 1) | 1u);
      if (lane == 0) *(LAS u32x2*)(L + L_TAG) = u32x2{tA, tB};
    }
    STAMP(1);
    BAR();
    STAMP(2);
    tA = *(LAS u32*)(L + L_TAG);
    tB = *(LAS u32*)(L + L_TAG + 4);
    BAR();
    STAMP(3);
    if (cb == 0) {
      f32x4 a = *(LAS f32x4*)(L + L_QP + lane * 16), a2 = *(LAS f32x4*)(L + L_QP + 1024 + lane * 16);
      a += a2;
      a.x = xrow_sum(a.x); a.y = xrow_sum(a.y); a.z = xrow_sum(a.z); a.w = xrow_sum(a.w);
      if (lane < 16) stmb(rs, WS_Q + ((x * 4 + (lane >> 2)) * 128 + 4 * j + (lane & 3)) * 16, u32x4{pk(a.x, a.y), tA, pk(a.z, a.w), tB});
      STAMP(4);
      const u32 qo = WS_Q + ((x * 4 + (j >> 3)) * 128 + lane) * 16;
      u32x4 g0, g1;
      for (int it = 0; it < SPIN_CAP; ++it) {
        asm volatile("" ::: "memory");
        g0 = ldmb(rs, qo);
        g1 = ldmb(rs, qo + 1024);
        if (allok(tagok(g0, tA, tB) && tagok(g1, tA, tB))) break;
      }
      *(LAS u32x2*)(L + L_QI + (lane >> 5) * QI_S + (lane & 31) * 8) = u32x2{g0.x, g0.z};
      *(LAS u32x2*)(L + L_QI + (2 + (lane >> 5)) * QI_S + (lane & 31) * 8) = u32x2{g1.x, g1.z};
      STAMP(5);
    } else if (cb == 1 && ffn) {
      f32x4 a = *(LAS f32x4*)(L + L_GP + lane * 16), a2 = *(LAS f32x4*)(L + L_GP + 1024 + lane * 16);
      a += a2;
      a.x = xrow_sum(a.x); a.y = xrow_sum(a.y); a.z = xrow_sum(a.z); a.w = xrow_sum(a.w);
      const int lg = 7 * x + (j >> 2);
      if (lane < 16) stmb(rs, WS_G + ((lg * 4 + (lane >> 2)) * 16 + 4 * (j & 3) + (lane & 3)) * 16, u32x4{pk(a.x, a.y), tA, pk(a.z, a.w), tB});
    }
    BAR();
    BAR();
    BAR();
    STAMP(6);
    {
      const u32 so = WS_S + ((x * 32 + 8 * cb + 2 * g) * 4 + (n16 & 3)) * 16;
      const u32 oo = WS_O + (((x * 8 + (j >> 2)) * 32 + 8 * cb + 2 * g) * 16 + n16) * 16;
      u32x4 sa, sb, da, db;
      if (cb == 1 && ffn) {
        const u32 go = WS_G + ((2 * j) * 64 + lane) * 16;
        u32x4 gg, uu;
        for (int it = 0; it < SPIN_CAP; ++it) {
          asm volatile("" ::: "memory");
          gg = ldmb(rs, go);
          uu = ldmb(rs, go + 1024);
          if (allok(tagok(gg, tA, tB) && tagok(uu, tA, tB))) break;
        }
        const float g0 = bflo(gg.x), g1 = bfhi(gg.x), g2 = bflo(gg.z), g3 = bfhi(gg.z);
        const float h0 = g0 * __builtin_amdgcn_rcpf(1.f + __builtin_amdgcn_exp2f(-g0 * LOG2E)) * bflo(uu.x);
        const float h1 = g1 * __builtin_amdgcn_rcpf(1.f + __builtin_amdgcn_exp2f(-g1 * LOG2E)) * bfhi(uu.x);
        const float h2 = g2 * __builtin_amdgcn_rcpf(1.f + __builtin_amdgcn_exp2f(-g2 * LOG2E)) * bflo(uu.z);
        const float h3 = g3 * __builtin_amdgcn_rcpf(1.f + __builtin_amdgcn_exp2f(-g3 * LOG2E)) * bfhi(uu.z);
        *(LAS u32x2*)(L + L_HA + g * OA_S + (n16 >> 3) * 64 + (n16 & 3) * 16 + ((n16 & 7) >> 2) * 8) = u32x2{pk(h0, h1), pk(h2, h3)};
      }
      for (int it = 0; it < SPIN_CAP; ++it) {
        asm volatile("" ::: "memory");
        sa = ldmb(rs, so); sb = ldmb(rs, so + 64);
        da = ldmb(rs, oo); db = ldmb(rs, oo + 256);
        if (allok(tagok(sa, tA, tB) && tagok(sb, tA, tB) && tagok(da, tA, tB) && tagok(db, tA, tB))) break;
      }
      STAMP(7);
      const float Ma = __uint_as_float(sa.x), La = __uint_as_float(sa.z), Mb = __uint_as_float(sb.x), Lb = __uint_as_float(sb.z);
      const float Mx = xrow_max(fmaxf(Ma, Mb));
      const float ea = __builtin_amdgcn_exp2f(Ma - Mx), eb = __builtin_amdgcn_exp2f(Mb - Mx);
      const float r = 1.0f / xrow_sum(fmaf(ea, La, eb * Lb));
      const u32 wp = pk(ea * r, eb * r);
      const u32 sh = (n16 & 1) * 16;
      const u32 A_ = (wp & 0xffffu) << sh, B_ = (wp >> 16) << sh;
      const bool lo = (n16 & 2) == 0;
      const u32x4 wf = {lo ? A_ : 0u, lo ? 0u : A_, lo ? B_ : 0u, lo ? 0u : B_};
      f32x4 d = {0.f, 0.f, 0.f, 0.f};
      d = mfma(u32x4{da.x, da.z, db.x, db.z}, wf, d);
      if (n16 < 4) *(LAS u32x2*)(L + L_OA + cb * OA_S + (16 * n16 + 4 * g) * 2) = u32x2{pk(d.x, d.y), pk(d.z, d.w)};
    }
    STAMP(8);
    BAR();
    if (cb == 0) {
      const u32 ho = WS_H + (c * 96 * 4 + lane) * 16;
      u32x4 h[6];
      const bool v5 = lane < 48;
      for (int it = 0; it < SPIN_CAP; ++it) {
        asm volatile("" ::: "memory");
        bool ok = true;
#pragma unroll
        for (int i = 0; i < 5; ++i) h[i] = ldmb(rs, ho + i * 1024);
        h[5] = ldmb(rs, ho + (v5 ? 5 : 4) * 1024);
#pragma unroll
        for (int i = 0; i < 6; ++i) ok = ok && tagok(h[i], tA, tB);
        if (allok(ok)) break;
      }
      STAMP(9);
      float s0 = 0.f, s1 = 0.f;
#pragma unroll
      for (int i = 0; i < 4; ++i) { s0 += __uint_as_float(h[i].x); s1 += __uint_as_float(h[i].z); }
      float f0 = __uint_as_float(h[4].x) + (v5 ? __uint_as_float(h[5].x) : 0.f);
      float f1 = __uint_as_float(h[4].z) + (v5 ? __uint_as_float(h[5].z) : 0.f);
      s0 += dpp<0x124>(s0); s1 += dpp<0x124>(s1); f0 += dpp<0x124>(f0); f1 += dpp<0x124>(f1);
      s0 += dpp<0x128>(s0); s1 += dpp<0x128>(s1); f0 += dpp<0x128>(f0); f1 += dpp<0x128>(f1);
      s0 = xrow_sum(s0); s1 = xrow_sum(s1); f0 = xrow_sum(f0); f1 = xrow_sum(f1);
      const float y0 = bflo(xr0) + s0 * (f0 * __builtin_amdgcn_rcpf(1.f + __builtin_amdgcn_exp2f(-f0 * LOG2E)));
      const float y1 = bflo(xr1) + s1 * (f1 * __builtin_amdgcn_rcpf(1.f + __builtin_amdgcn_exp2f(-f1 * LOG2E)));
      if (lane < 4) {
        const u32 y = pk(y0, y1);
        ((GAS u16*)out)[(2 * bp) * 512 + 4 * cq + lane] = (u16)y;
        ((GAS u16*)out)[(2 * bp + 1) * 512 + 4 * cq + lane] = (u16)(y >> 16);
      }
      if (c == 0 && lane == 0) __hip_atomic_store((GAS u64*)ws, T, __ATOMIC_RELAXED, __HIP_MEMORY_SCOPE_AGENT);
      STAMP(10);
    }
    STAMP_FLUSH();
  }
}

extern "C" void kernel_launch(void* const* d_in, const int* in_sizes, int n_in, void* d_out, int out_size, void* d_ws, size_t ws_size,
                              hipStream_t stream) {
  fused_decode_kernel<<<256, 1024, 0, stream>>>((const u16*)d_in[0], (const u16*)d_in[1], (const u16*)d_in[2], (const u16*)d_in[3],
                                               (const u16*)d_in[4], (const u16*)d_in[6], (const u16*)d_in[7], (const u16*)d_in[8],
                                               (u16*)d_out, d_ws);
}
```

```cpp
#define PRER 3
#define D2 12
#define D3 8
#define WS_BASE (250u << 20)
#include <hip/hip_runtime.h>
#include <stdint.h>

namespace {
typedef unsigned short u16;
typedef unsigned u32;
typedef unsigned long long u64;
typedef u32 u32x4 __attribute__((ext_vector_type(4)));
typedef u32 u32x2 __attribute__((ext_vector_type(2)));
typedef float f32x4 __attribute__((ext_vector_type(4)));
typedef float f32x2 __attribute__((ext_vector_type(2)));
typedef short s16x4 __attribute__((ext_vector_type(4)));
typedef __bf16 bf16x2 __attribute__((ext_vector_type(2)));
typedef __bf16 bf16x8 __attribute__((ext_vector_type(8)));
#define LAS __attribute__((address_space(3)))
#define GAS __attribute__((address_space(1)))

constexpr u32 WS_EPOCH = WS_BASE;
constexpr u32 WS_Q = WS_BASE + 4096;
constexpr u32 WS_G = WS_Q + 262144;
constexpr u32 WS_S = WS_G + 229376;
constexpr u32 WS_O = WS_S + 131072;
constexpr u32 WS_H = WS_O + 524288;
constexpr u32 WS_END = WS_H + 256 * 96 * 32;
constexpr u32 WS_DBG = (WS_END + 4095) & ~4095u;

constexpr u32 L_WQ = 0, L_WG = 16384, L_K = 32768, L_V = 65536, L_WO = 98304, L_W2 = 114688;
constexpr u32 L_XN = 122880, XN_S = 1088;
constexpr u32 L_XR = L_XN + 4 * XN_S;
constexpr u32 L_QI = L_XR + 4096, QI_S = 320;
constexpr u32 L_PI = L_QI + 4 * QI_S;
constexpr u32 L_MX = L_PI + 4 * QI_S;
constexpr u32 L_OA = L_MX + 128, OA_S = 192;
constexpr u32 L_HA = L_OA + 4 * OA_S;
constexpr u32 L_TAG = L_HA + 4 * OA_S;
constexpr u32 L_END = L_TAG + 16;

constexpr float SM_C = 0.08838834764831845f * 1.4426950408889634f;
constexpr float LOG2E = 1.4426950408889634f;
constexpr int SPIN_CAP = 1 << 16;
constexpr int AUX_SC1 = 16;

#define KV_POL 1
#define W_POL 1
#define ROT 1
#define SCH 2
#define SREP 0
#define PREW 0
#define D1 0
#define POLSTR_0 ""
#define POLSTR_1 " nt"
#define POLCAT(x) POLSTR_##x
#define POLSTR(x) POLCAT(x)

__device__ __forceinline__ void dma_kv(const void* sbase, u32 voff, u32 lds) {
  asm volatile("s_mov_b32 m0, %2\n\ts_nop 4\n\tglobal_load_lds_dwordx4 %0, %1" POLSTR(KV_POL)::"v"(voff), "s"(sbase), "s"(lds) : "memory");
}
__device__ __forceinline__ void dma_w(const void* sbase, u32 voff, u32 lds) {
  asm volatile("s_mov_b32 m0, %2\n\ts_nop 4\n\tglobal_load_lds_dwordx4 %0, %1" POLSTR(W_POL)::"v"(voff), "s"(sbase), "s"(lds) : "memory");
}
#define VMWAIT(n) asm volatile("s_waitcnt vmcnt(" #n ")" ::: "memory")
#define BAR() asm volatile("s_waitcnt lgkmcnt(0)\n\ts_barrier" ::: "memory")

__device__ __forceinline__ u32 pk(float lo, float hi) {
  f32x2 v = {lo, hi};
  return __builtin_bit_cast(u32, __builtin_convertvector(v, bf16x2));
}
__device__ __forceinline__ float bflo(u32 v) { return __uint_as_float(v << 16); }
__device__ __forceinline__ float bfhi(u32 v) { return __uint_as_float(v & 0xffff0000u); }
__device__ __forceinline__ f32x4 mfma(u32x4 a, u32x4 b, f32x4 c) {
  return __builtin_amdgcn_mfma_f32_16x16x32_bf16(__builtin_bit_cast(bf16x8, a), __builtin_bit_cast(bf16x8, b), c, 0, 0, 0);
}
__device__ __forceinline__ u32x4 lds128(LAS unsigned char* L, u32 off) { return *(LAS u32x4*)(L + off); }
__device__ __forceinline__ u32x4 tr2(LAS unsigned char* L, u32 off0, u32 off1) {
  u32x2 a = __builtin_bit_cast(u32x2, __builtin_amdgcn_ds_read_tr16_b64_v4i16((LAS s16x4*)(L + off0)));
  u32x2 b = __builtin_bit_cast(u32x2, __builtin_amdgcn_ds_read_tr16_b64_v4i16((LAS s16x4*)(L + off1)));
  return u32x4{a.x, a.y, b.x, b.y};
}
template <int CTRL> __device__ __forceinline__ float dpp(float x) {
  return __builtin_bit_cast(float, __builtin_amdgcn_mov_dpp(__builtin_bit_cast(int, x), CTRL, 0xf, 0xf, true));
}
__device__ __forceinline__ float vmax(float a, float b) { float r; asm("v_max_f32 %0, %1, %2" : "=v"(r) : "v"(a), "v"(b)); return r; }
__device__ __forceinline__ float vmax3(float a, float b, float c) { float r; asm("v_max3_f32 %0, %1, %2, %3" : "=v"(r) : "v"(a), "v"(b), "v"(c)); return r; }
__device__ __forceinline__ float xrow_sum(float x) {
  auto s = __builtin_amdgcn_permlane16_swap(__float_as_uint(x), __float_as_uint(x), false, false);
  x = __uint_as_float(s[0]) + __uint_as_float(s[1]);
  auto t = __builtin_amdgcn_permlane32_swap(__float_as_uint(x), __float_as_uint(x), false, false);
  return __uint_as_float(t[0]) + __uint_as_float(t[1]);
}
__device__ __forceinline__ float x32_sum(float x) {
  auto t = __builtin_amdgcn_permlane32_swap(__float_as_uint(x), __float_as_uint(x), false, false);
  return __uint_as_float(t[0]) + __uint_as_float(t[1]);
}
__device__ __forceinline__ float xrow_max(float x) {
  auto s = __builtin_amdgcn_permlane16_swap(__float_as_uint(x), __float_as_uint(x), false, false);
  x = vmax(__uint_as_float(s[0]), __uint_as_float(s[1]));
  auto t = __builtin_amdgcn_permlane32_swap(__float_as_uint(x), __float_as_uint(x), false, false);
  return vmax(__uint_as_float(t[0]), __uint_as_float(t[1]));
}
__device__ __forceinline__ float wave_sum(float x) {
  x += dpp<0xB1>(x);
  x += dpp<0x4E>(x);
  x += dpp<0x141>(x);
  x += dpp<0x140>(x);
  return xrow_sum(x);
}
__device__ __forceinline__ float sigm(float v) { return __builtin_amdgcn_rcpf(1.f + __builtin_amdgcn_exp2f(-v * LOG2E)); }
__device__ __forceinline__ u32x4 ldmb(__amdgpu_buffer_rsrc_t rs, u32 off) { return __builtin_amdgcn_raw_buffer_load_b128(rs, (int)off, 0, AUX_SC1); }
__device__ __forceinline__ void stmb(__amdgpu_buffer_rsrc_t rs, u32 off, u32x4 v) { __builtin_amdgcn_raw_buffer_store_b128(v, rs, (int)off, 0, AUX_SC1); }
__device__ __forceinline__ u32 tagbad(u32x4 v, u32 tA, u32 tB) { return (v[1] ^ tA) | (v[3] ^ tB); }
template <int N> __device__ __forceinline__ void gather(__amdgpu_buffer_rsrc_t rs, const u32 (&off)[N], u32x4 (&v)[N], u32 tA, u32 tB, bool need) {
  for (int it = 0; it < SPIN_CAP; ++it) {
    if (__builtin_amdgcn_ballot_w64(need) == 0ull) break;
    asm volatile("" ::: "memory");
    if (need) {
#pragma unroll
      for (int i = 0; i < N; ++i) v[i] = ldmb(rs, off[i]);
      u32 bad = 0;
#pragma unroll
      for (int i = 0; i < N; ++i) bad |= tagbad(v[i], tA, tB);
      need = bad != 0;
    }
  }
}
template <int N> __device__ __forceinline__ void nap() { if (N > 0) __builtin_amdgcn_s_sleep(N); }

#define STAMP(k)
#define STAMP_DECL
#define STAMP_FLUSH()
}

__global__ void __launch_bounds__(768) fused_decode_kernel(const u16* __restrict__ xp, const u16* __restrict__ kc, const u16* __restrict__ vc,
                                                           const u16* __restrict__ wq, const u16* __restrict__ wo, const u16* __restrict__ w1,
                                                           const u16* __restrict__ w3, const u16* __restrict__ w2, u16* __restrict__ out,
                                                           void* __restrict__ ws) {
  __shared__ __attribute__((aligned(16))) unsigned char smem[L_END];
  LAS unsigned char* L = (LAS unsigned char*)smem;
  const u32 lb = (u32)(uintptr_t)L;
  const int tid = threadIdx.x, lane = tid & 63;
  const int w = __builtin_amdgcn_readfirstlane(tid >> 6);
  const int c = blockIdx.x, x = c & 7, j = c >> 3;
  const bool ffn = j < 28;
  const int n16 = lane & 15, g = lane >> 4;
  const __amdgpu_buffer_rsrc_t rs = __builtin_amdgcn_make_buffer_rsrc(ws, 0, (int)WS_END, 0x00020000);
  const int lg = 7 * x + (j >> 2), mtx = lg & 1, G1 = lg >> 1, kq = j & 3, lgq = j >> 2;
  STAMP_DECL;
  STAMP(0);

  if (w < 8) {
    const int b = j >> 3, s = j & 7;
    const int wr = ROT ? ((w + j + x) & 7) : w;
    const size_t kvb = ((size_t)((b * 8 + x) * 4096 + 3072 + 128 * s + 16 * wr)) * 256;
    const char* kb = (const char*)kc + kvb;
    const char* vb = (const char*)vc + kvb;
    const u32 sw128 = ((((lane & 7) >> 1) ^ g) * 32) + (lane & 1) * 16;
    {
      const char* sb = (const char*)wq + (size_t)(128 * kq + 16 * wr) * 8192 + (512 * x + 64 * lgq) * 2;
      const u32 vo = (u32)(lane >> 3) * 8192 + sw128;
      dma_w(sb, vo, lb + L_WQ + (16 * wr) * 128);
      dma_w(sb + 8 * 8192, vo, lb + L_WQ + (16 * wr + 8) * 128);
    }
    if (ffn) {
      const char* sb = (const char*)(mtx ? w3 : w1) + (size_t)(128 * kq + 16 * wr) * 3584 + (64 * G1) * 2;
      const u32 vo = (u32)(lane >> 3) * 3584 + sw128;
      dma_w(sb, vo, lb + L_WG + (16 * wr) * 128);
      dma_w(sb + 8 * 3584, vo, lb + L_WG + (16 * wr + 8) * 128);
    }
    auto issue_k = [&](int i) { const u32 r = 4 * i + g; dma_kv(kb, r * 256 + ((n16 ^ r) * 16), lb + L_K + (16 * wr + 4 * i) * 256); };
    auto issue_v = [&](int i) { dma_kv(vb, (4 * i + g) * 256 + ((n16 ^ ((g << 2) | i)) * 16), lb + L_V + (16 * wr + 4 * i) * 256); };
    STAMP(1);
    BAR();
    const u32 tA = *(LAS u32*)(L + L_TAG), tB = *(LAS u32*)(L + L_TAG + 4);
    const int q4 = n16 >> 2, p4 = lane & 3, t4 = w & 3;
    u32x4 xf[4];
#pragma unroll
    for (int st = 0; st < 4; ++st) xf[st] = lds128(L, L_XN + (n16 & 3) * XN_S + (4 * kq + st) * 64 + g * 16);
    const u32 tro = (4 * g + q4) * 128 + (u32)(t4 ^ ((2 * g + (q4 >> 1)) & 3)) * 32 + 8 * p4;
    const int bsrc = ((lane >> 2) + 16 * (lane & 3)) * 4;
    if (ffn) VMWAIT(2); else VMWAIT(0);
    STAMP(2);
    BAR();
    if (w < 4) {
      f32x4 acc = {0.f, 0.f, 0.f, 0.f};
#pragma unroll
      for (int st = 0; st < 4; ++st) acc = mfma(tr2(L, L_WQ + st * 4096 + tro, L_WQ + st * 4096 + 2048 + tro), xf[st], acc);
      const u32 d0 = __builtin_amdgcn_ds_bpermute(bsrc, pk(acc.x, acc.y)), d1 = __builtin_amdgcn_ds_bpermute(bsrc, pk(acc.z, acc.w));
      if (lane < 16) stmb(rs, WS_Q + (((((x * 4 + (lane >> 2)) * 4 + (lgq >> 1)) * 4 + kq) * 32) + 16 * (lgq & 1) + 4 * t4 + (lane & 3)) * 16, u32x4{d0, tA, d1, tB});
    }
    STAMP(3);
    issue_k(0); issue_k(1); issue_k(2); issue_k(3);
    VMWAIT(4);
    STAMP(4);
    BAR();
    if (ffn && w >= 4) {
      f32x4 acc = {0.f, 0.f, 0.f, 0.f};
#pragma unroll
      for (int st = 0; st < 4; ++st) acc = mfma(tr2(L, L_WG + st * 4096 + tro, L_WG + st * 4096 + 2048 + tro), xf[st], acc);
      const u32 d0 = __builtin_amdgcn_ds_bpermute(bsrc, pk(acc.x, acc.y)), d1 = __builtin_amdgcn_ds_bpermute(bsrc, pk(acc.z, acc.w));
      if (lane < 16) stmb(rs, WS_G + ((((G1 * 4 + (lane >> 2)) * 2 + mtx) * 4 + kq) * 16 + 4 * t4 + (lane & 3)) * 16, u32x4{d0, tA, d1, tB});
    }
    issue_v(0); issue_v(1); issue_v(2); issue_v(3);
    VMWAIT(4);
    STAMP(5);
    u32x4 kf[4];
#pragma unroll
    for (int st = 0; st < 4; ++st) kf[st] = lds128(L, L_K + (16 * wr + n16) * 256 + (((4 * st + g) ^ n16) * 16));
    BAR();
    STAMP(6);
    f32x4 sc = {0.f, 0.f, 0.f, 0.f};
#pragma unroll
    for (int st = 0; st < 4; ++st) sc = mfma(kf[st], lds128(L, L_QI + (n16 & 3) * QI_S + st * 64 + g * 16), sc);
    const float mx = xrow_max(vmax3(sc.x, sc.y, vmax(sc.z, sc.w)));
    if (lane < 4) *(LAS float*)(L + L_MX + lane * 32 + w * 4) = mx;
    BAR();
    float Mc;
    {
      const f32x4 m0 = *(LAS f32x4*)(L + L_MX + (n16 & 3) * 32), m1 = *(LAS f32x4*)(L + L_MX + (n16 & 3) * 32 + 16);
      Mc = vmax3(vmax3(m0.x, m0.y, m0.z), vmax3(m0.w, m1.x, m1.y), vmax(m1.z, m1.w)) * SM_C;
      const float p0 = __builtin_amdgcn_exp2f(fmaf(sc.x, SM_C, -Mc)), p1 = __builtin_amdgcn_exp2f(fmaf(sc.y, SM_C, -Mc));
      const float p2 = __builtin_amdgcn_exp2f(fmaf(sc.z, SM_C, -Mc)), p3 = __builtin_amdgcn_exp2f(fmaf(sc.w, SM_C, -Mc));
      if (n16 < 4) *(LAS u32x2*)(L + L_PI + n16 * QI_S + (16 * wr + 4 * g) * 2) = u32x2{pk(p0, p1), pk(p2, p3)};
    }
    if (ffn) {
      const char* sb = (const char*)w2 + (size_t)(64 * j + 8 * wr) * 1024 + x * 128;
      dma_w(sb, (u32)(lane >> 3) * 1024 + sw128, lb + L_W2 + 8 * wr * 128);
    }
    {
      const int dt = j >> 2, cg4 = j & 3;
      const char* sb = (const char*)wo + (size_t)((4 * x + (wr >> 1)) * 128 + 16 * dt + 8 * (wr & 1)) * 1024 + cg4 * 256;
#pragma unroll
      for (int i = 0; i < 2; ++i)
        dma_w(sb, (u32)(4 * i + g) * 1024 + ((n16 ^ ((g << 2) | ((2 * wr + i) & 3))) * 16), lb + L_WO + (8 * wr + 4 * i) * 256);
    }
    if (ffn) VMWAIT(3); else VMWAIT(2);
    STAMP(7);
    BAR();
    const u32 ch = 2 * w + (p4 >> 1);
    const u32 ta0 = (8 * g + q4) * 256 + ((ch ^ ((q4 << 2) | ((2 * g) & 3))) * 16) + 8 * (p4 & 1);
    const u32 ta1 = (8 * g + q4 + 4) * 256 + ((ch ^ ((q4 << 2) | ((2 * g + 1) & 3))) * 16) + 8 * (p4 & 1);
    {
      f32x4 o = {0.f, 0.f, 0.f, 0.f}, l = {0.f, 0.f, 0.f, 0.f};
      const u32x4 ones = {0x3F803F80u, 0x3F803F80u, 0x3F803F80u, 0x3F803F80u};
#pragma unroll
      for (int st = 0; st < 4; ++st) {
        const u32x4 pf = lds128(L, L_PI + (n16 & 3) * QI_S + st * 64 + g * 16);
        o = mfma(pf, tr2(L, L_V + st * 8192 + ta0, L_V + st * 8192 + ta1), o);
        l = mfma(pf, ones, l);
      }
      if (lane < 16) stmb(rs, WS_O + (((x * 8 + w) * 32 + j) * 16 + lane) * 16, u32x4{pk(o.x, o.y), tA, pk(o.z, o.w), tB});
      if (w == 0 && lane < (SREP ? 32 : 4)) {
        const int hh = lane & 3;
        const float lv = hh == 0 ? l.x : hh == 1 ? l.y : hh == 2 ? l.z : l.w;
        stmb(rs, WS_S + (((x * 8 + (lane >> 2)) * 32 + j) * 4 + hh) * 16, u32x4{__float_as_uint(Mc), tA, __float_as_uint(lv), tB});
      }
    }
    STAMP(8);
    if (w == 0) VMWAIT(4); else VMWAIT(3);
    BAR();
    if (ffn && w < 4) {
      f32x4 f = {0.f, 0.f, 0.f, 0.f};
#pragma unroll
      for (int st = 0; st < 2; ++st)
        f = mfma(lds128(L, L_HA + (n16 & 3) * OA_S + st * 64 + g * 16), tr2(L, L_W2 + st * 4096 + tro, L_W2 + st * 4096 + 2048 + tro), f);
      if (lane < 16) {
        const int col = 64 * x + 16 * w + lane;
        stmb(rs, WS_H + (((col >> 1) * 96 + 64 + j) * 2 + (col & 1)) * 16, u32x4{pk(f.x, f.y), tA, pk(f.z, f.w), tB});
      }
      if (w == 0) VMWAIT(3); else VMWAIT(2);
    } else {
      if (w == 0) VMWAIT(2); else VMWAIT(1);
    }
    STAMP(9);
    BAR();
    STAMP(10);
    {
      f32x4 a = {0.f, 0.f, 0.f, 0.f};
#pragma unroll
      for (int st = 0; st < 2; ++st)
        a = mfma(lds128(L, L_OA + (n16 & 3) * OA_S + st * 64 + g * 16), tr2(L, L_WO + st * 8192 + ta0, L_WO + st * 8192 + ta1), a);
      if (lane < 16) {
        const int col = 128 * (j & 3) + 16 * w + lane, slot = x * 8 + (j >> 2);
        stmb(rs, WS_H + (((col >> 1) * 96 + slot) * 2 + (col & 1)) * 16, u32x4{pk(a.x, a.y), tA, pk(a.z, a.w), tB});
      }
    }
    STAMP(11);
    BAR();
    STAMP_FLUSH();
  } else {
    __builtin_amdgcn_s_setprio(3);
    const int cb = w - 8;
    const u32 one = (n16 & 1) ? 0x3F800000u : 0x3F80u;
    const bool lo2 = (n16 & 2) == 0;
    const f32x4 z4 = {0.f, 0.f, 0.f, 0.f};
    const u32x4 xv = *(const GAS u32x4*)(xp + cb * 512 + lane * 8);
    u64 E = 0;
    if (cb == 0) E = __hip_atomic_load((GAS u64*)((char*)ws + WS_EPOCH), __ATOMIC_RELAXED, __HIP_MEMORY_SCOPE_AGENT);
    {
      *(LAS u32x4*)(L + L_XR + cb * 1024 + lane * 16) = xv;
      float v[8] = {bflo(xv[0]), bfhi(xv[0]), bflo(xv[1]), bfhi(xv[1]), bflo(xv[2]), bfhi(xv[2]), bflo(xv[3]), bfhi(xv[3])};
      float sm = ((v[0] + v[1]) + (v[2] + v[3])) + ((v[4] + v[5]) + (v[6] + v[7]));
      const float mean = wave_sum(sm) * (1.0f / 512.0f);
      float sq = 0.f;
#pragma unroll
      for (int e = 0; e < 8; ++e) { v[e] -= mean; sq = fmaf(v[e], v[e], sq); }
      const float rstd = __builtin_amdgcn_rsqf(wave_sum(sq) * (1.0f / 512.0f) + 1e-5f);
      const u32 o = L_XN + cb * XN_S + (lane >> 2) * 64 + (lane & 1) * 32 + ((lane & 3) >> 1) * 8;
      *(LAS u32x2*)(L + o) = u32x2{pk(v[0] * rstd, v[1] * rstd), pk(v[2] * rstd, v[3] * rstd)};
      *(LAS u32x2*)(L + o + 16) = u32x2{pk(v[4] * rstd, v[5] * rstd), pk(v[6] * rstd, v[7] * rstd)};
    }
    u64 T = 0;
    if (cb == 0) {
      T = E + 1;
      const u32 a = (u32)T;
      if (lane == 0) *(LAS u32x2*)(L + L_TAG) = u32x2{a, (u32)(E >> 32) ^ ((a << 1) | 1u)};
    }
    STAMP(1);
    BAR();
    const u32 tA = *(LAS u32*)(L + L_TAG), tB = *(LAS u32*)(L + L_TAG + 4);
    u32x4 pr0, pr1;
    {
      const u32 ho = WS_H + (c * 96 * 2 + lane) * 16, oo = WS_O + ((x * 8 + (j >> 2)) * 512 + (j & 3) * 128 + lane) * 16;
      const u32 so = SREP ? WS_S + ((x * 8 + (j >> 2)) * 128 + (j & 3) * 32 + (lane & 31)) * 16 : WS_S + ((x * 8) * 128 + j * 4 + (lane & 3)) * 16, oob = 0x7ffffff0u;
      const u32 a0 = cb == 0 ? ((PRER & 1) ? ho : oob) : cb == 1 ? ((PRER & 1) ? ho + 2048 : oob) : cb == 2 ? ((PRER & 2) ? oo : oob) : oob;
      const u32 a1 = cb == 0 ? ((PRER & 1) ? ho + 1024 : oob) : cb == 1 ? ((PRER & 2) ? so : oob) : cb == 2 ? ((PRER & 2) ? oo + 1024 : oob) : oob;
      pr0 = ldmb(rs, a0); pr1 = ldmb(rs, a1);
    }
    STAMP(2);
    BAR();
    BAR();
    STAMP(3);
    {
      nap<D1>();
      const u32 qo = WS_Q + ((((x * 4 + (j >> 3)) * 4 + cb) * 4) * 32 + lane) * 16;
      const u32 off[2] = {qo, qo + 1024};
      u32x4 v[2];
      gather<2>(rs, off, v, tA, tB, true);
      const bool on = (g & 1) == ((n16 >> 2) & 1);
      const u32 d01 = (on && lo2) ? one : 0u, d23 = (on && !lo2) ? one : 0u;
      const f32x4 q = mfma(u32x4{d01, d23, d01, d23}, u32x4{v[0][0], v[0][2], v[1][0], v[1][2]}, z4);
      if (lane < 32) *(LAS u32x2*)(L + L_QI + cb * QI_S + lane * 8) = u32x2{pk(q.x, q.y), pk(q.z, q.w)};
    }
    asm volatile("" :: "v"(pr0), "v"(pr1));
    STAMP(4);
    BAR();
    BAR();
    const u32 go = WS_G + (((j * 4 + cb) * 2) * 64 + lane) * 16;
    u32x4 gv[2];
    if (ffn) { gv[0] = ldmb(rs, go); gv[1] = ldmb(rs, go + 1024); }
    BAR();
    STAMP(5);
    if (ffn) {
      if (__builtin_amdgcn_ballot_w64((tagbad(gv[0], tA, tB) | tagbad(gv[1], tA, tB)) != 0) != 0ull) {
        const u32 off[2] = {go, go + 1024};
        gather<2>(rs, off, gv, tA, tB, true);
      }
      const int dw = (n16 & 7) >> 1;
      const f32x4 d = mfma(u32x4{dw == 0 ? one : 0u, dw == 1 ? one : 0u, dw == 2 ? one : 0u, dw == 3 ? one : 0u},
                           u32x4{gv[0][0], gv[0][2], gv[1][0], gv[1][2]}, z4);
      const auto s0 = __builtin_amdgcn_permlane16_swap(__float_as_uint(d.x), __float_as_uint(d.x), false, false);
      const auto s1 = __builtin_amdgcn_permlane16_swap(__float_as_uint(d.y), __float_as_uint(d.y), false, false);
      const auto s2 = __builtin_amdgcn_permlane16_swap(__float_as_uint(d.z), __float_as_uint(d.z), false, false);
      const auto s3 = __builtin_amdgcn_permlane16_swap(__float_as_uint(d.w), __float_as_uint(d.w), false, false);
      const float g0 = __uint_as_float(s0[0]), g1 = __uint_as_float(s1[0]), g2 = __uint_as_float(s2[0]), g3 = __uint_as_float(s3[0]);
      const float u0 = __uint_as_float(s0[1]), u1 = __uint_as_float(s1[1]), u2 = __uint_as_float(s2[1]), u3 = __uint_as_float(s3[1]);
      if (lane < 16)
        *(LAS u32x2*)(L + L_HA + cb * OA_S + (n16 >> 3) * 64 + (n16 & 3) * 16 + ((n16 & 7) >> 2) * 8) =
            u32x2{pk(g0 * sigm(g0) * u0, g1 * sigm(g1) * u1), pk(g2 * sigm(g2) * u2, g3 * sigm(g3) * u3)};
    }
    STAMP(6);
    BAR();
    {
      nap<D2>();
      const u32 so = WS_S + (((x * 8 + (SREP ? (j >> 2) : 0)) * 32 + 8 * cb + 2 * g) * 4 + (n16 & 3)) * 16;
      const u32 oo = WS_O + (((x * 8 + (j >> 2)) * 32 + 8 * cb + 2 * g) * 16 + n16) * 16;
      const u32 off[4] = {so, so + 64, oo, oo + 256};
      u32x4 v[4];
      gather<4>(rs, off, v, tA, tB, true);
      STAMP(7);
      const float Ma = __uint_as_float(v[0][0]), La = __uint_as_float(v[0][2]), Mb = __uint_as_float(v[1][0]), Lb = __uint_as_float(v[1][2]);
      const float Mx = xrow_max(vmax(Ma, Mb));
      const float ea = __builtin_amdgcn_exp2f(Ma - Mx), eb = __builtin_amdgcn_exp2f(Mb - Mx);
      const float r = __builtin_amdgcn_rcpf(xrow_sum(fmaf(ea, La, eb * Lb)));
      const u32 wp = pk(ea * r, eb * r);
      const u32 sh = (n16 & 1) * 16;
      const u32 A_ = (wp & 0xffffu) << sh, B_ = (wp >> 16) << sh;
      const bool lo = (n16 & 2) == 0;
      const u32x4 wf = {lo ? A_ : 0u, lo ? 0u : A_, lo ? B_ : 0u, lo ? 0u : B_};
      f32x4 d = {0.f, 0.f, 0.f, 0.f};
      d = mfma(u32x4{v[2][0], v[2][2], v[3][0], v[3][2]}, wf, d);
      if (n16 < 4) *(LAS u32x2*)(L + L_OA + cb * OA_S + (16 * n16 + 4 * g) * 2) = u32x2{pk(d.x, d.y), pk(d.z, d.w)};
    }
    STAMP(8);
    BAR();
    BAR();
    if (cb == 0) {
      nap<D3>();
      const u32 ho = WS_H + (c * 96 * 2 + lane) * 16;
      const bool v2 = lane < 56;
      const u32 off[3] = {ho, ho + 1024, ho + (v2 ? 2048u : 0u)};
      u32x4 h[3];
      gather<3>(rs, off, h, tA, tB, true);
      STAMP(9);
      const u32x4 sel = {lo2 ? one : 0u, lo2 ? 0u : one, lo2 ? one : 0u, lo2 ? 0u : one};
      const f32x4 ds = mfma(u32x4{h[0][0], h[0][2], h[1][0], h[1][2]}, sel, z4);
      const f32x4 df = mfma(u32x4{v2 ? h[2][0] : 0u, v2 ? h[2][2] : 0u, 0u, 0u}, sel, z4);
      const float s0 = xrow_sum(ds.x + ds.z), s1 = xrow_sum(ds.y + ds.w), f0 = xrow_sum(df.x + df.z), f1 = xrow_sum(df.y + df.w);
      if (lane < 4) {
        const u32 xr = *(LAS u32*)(L + L_XR + lane * 1024 + c * 4);
        const float y0 = fmaf(s0, f0 * sigm(f0), bflo(xr)), y1 = fmaf(s1, f1 * sigm(f1), bfhi(xr));
        ((GAS u32*)out)[lane * 256 + c] = pk(y0, y1);
      }
      if (c == 0 && lane == 0) __hip_atomic_store((GAS u64*)((char*)ws + WS_EPOCH), T, __ATOMIC_RELAXED, __HIP_MEMORY_SCOPE_AGENT);
      STAMP(10);
    }
    STAMP_FLUSH();
  }
}

extern "C" void kernel_launch(void* const* d_in, const int* in_sizes, int n_in, void* d_out, int out_size, void* d_ws, size_t ws_size,
                              hipStream_t stream) {
  fused_decode_kernel<<<256, 768, 0, stream>>>((const u16*)d_in[0], (const u16*)d_in[1], (const u16*)d_in[2], (const u16*)d_in[3],
                                               (const u16*)d_in[4], (const u16*)d_in[6], (const u16*)d_in[7], (const u16*)d_in[8],
                                               (u16*)d_out, d_ws);
}
```

```cpp
#define PRER 3
#define D1 8
#define D2 16
#define D3 8
#define DB2 0
#include <hip/hip_runtime.h>
#include <stdint.h>

namespace {
typedef unsigned short u16;
typedef unsigned u32;
typedef unsigned long long u64;
typedef u32 u32x4 __attribute__((ext_vector_type(4)));
typedef u32 u32x2 __attribute__((ext_vector_type(2)));
typedef float f32x4 __attribute__((ext_vector_type(4)));
typedef float f32x2 __attribute__((ext_vector_type(2)));
typedef short s16x4 __attribute__((ext_vector_type(4)));
typedef __bf16 bf16x2 __attribute__((ext_vector_type(2)));
typedef __bf16 bf16x8 __attribute__((ext_vector_type(8)));
#define LAS __attribute__((address_space(3)))
#define GAS __attribute__((address_space(1)))

#define WS_BASE 0u
constexpr u32 WS_EPOCH = WS_BASE;
constexpr u32 WS_Q = WS_BASE + 4096;
constexpr u32 WS_G = WS_Q + 262144;
constexpr u32 WS_O = WS_G + 229376;
constexpr u32 WS_H = WS_O + 786432;
constexpr u32 WS_END = WS_H + 256 * 96 * 32;
constexpr u32 WS_DBG = (WS_END + 4095) & ~4095u;

constexpr u32 L_WQ = 0, L_WG = 16384, L_K = 32768, L_V = 65536, L_WO = 98304, L_W2 = 114688;
constexpr u32 L_XN = 122880, XN_S = 1088;
constexpr u32 L_XR = L_XN + 4 * XN_S;
constexpr u32 L_QI = L_XR + 4096, QI_S = 320;
constexpr u32 L_PI = L_QI + 4 * QI_S;
constexpr u32 L_MX = L_PI + 4 * QI_S;
constexpr u32 L_OA = L_MX + 128, OA_S = 192;
constexpr u32 L_HA = L_OA + 4 * OA_S;
constexpr u32 L_TAG = L_HA + 4 * OA_S;
constexpr u32 L_END = L_TAG + 16;

constexpr float SM_C = 0.08838834764831845f * 1.4426950408889634f;
constexpr float LOG2E = 1.4426950408889634f;
constexpr int SPIN_CAP = 1 << 16;
constexpr int AUX_SC1 = 16;

#define KV_POL 1
#define W_POL 1
#define ROT 1
#define SCH 2
#define SREP 0
#define TOUCH 0
#define EXACT 1
#define B0 0
#define IPF 0
#define IPF_SPAN 1920
#define COMM_FIRST 1
#define XP 0
#define PRL 0
#define PREW 0
#define POLSTR_0 ""
#define POLSTR_1 " nt"
#define POLCAT(x) POLSTR_##x
#define POLSTR(x) POLCAT(x)

__device__ __forceinline__ void dma_kv(const void* sbase, u32 voff, u32 lds) {
  asm volatile("s_mov_b32 m0, %2\n\ts_nop 4\n\tglobal_load_lds_dwordx4 %0, %1" POLSTR(KV_POL)::"v"(voff), "s"(sbase), "s"(lds) : "memory");
}
__device__ __forceinline__ void dma_w(const void* sbase, u32 voff, u32 lds) {
  asm volatile("s_mov_b32 m0, %2\n\ts_nop 4\n\tglobal_load_lds_dwordx4 %0, %1" POLSTR(W_POL)::"v"(voff), "s"(sbase), "s"(lds) : "memory");
}
#define VMWAIT(n) asm volatile("s_waitcnt vmcnt(" #n ")" ::: "memory")
#define BAR() asm volatile("s_waitcnt lgkmcnt(0)\n\ts_barrier" ::: "memory")

__device__ __forceinline__ u32 pk(float lo, float hi) {
  f32x2 v = {lo, hi};
  return __builtin_bit_cast(u32, __builtin_convertvector(v, bf16x2));
}
__device__ __forceinline__ float bflo(u32 v) { return __uint_as_float(v << 16); }
__device__ __forceinline__ float bfhi(u32 v) { return __uint_as_float(v & 0xffff0000u); }
__device__ __forceinline__ f32x4 mfma(u32x4 a, u32x4 b, f32x4 c) {
  return __builtin_amdgcn_mfma_f32_16x16x32_bf16(__builtin_bit_cast(bf16x8, a), __builtin_bit_cast(bf16x8, b), c, 0, 0, 0);
}
__device__ __forceinline__ u32x4 lds128(LAS unsigned char* L, u32 off) { return *(LAS u32x4*)(L + off); }
__device__ __forceinline__ u32x4 tr2(LAS unsigned char* L, u32 off0, u32 off1) {
  u32x2 a = __builtin_bit_cast(u32x2, __builtin_amdgcn_ds_read_tr16_b64_v4i16((LAS s16x4*)(L + off0)));
  u32x2 b = __builtin_bit_cast(u32x2, __builtin_amdgcn_ds_read_tr16_b64_v4i16((LAS s16x4*)(L + off1)));
  return u32x4{a.x, a.y, b.x, b.y};
}
template <int CTRL> __device__ __forceinline__ float dpp(float x) {
  return __builtin_bit_cast(float, __builtin_amdgcn_mov_dpp(__builtin_bit_cast(int, x), CTRL, 0xf, 0xf, true));
}
__device__ __forceinline__ float pinf() { float r; asm("v_mov_b32 %0, 0x7f800000" : "=v"(r)); return r; }
__device__ __forceinline__ float vmax(float a, float b) { return __builtin_amdgcn_fmed3f(a, b, pinf()); }
__device__ __forceinline__ float vmax3(float a, float b, float c) { return vmax(vmax(a, b), c); }
__device__ __forceinline__ float xrow_sum(float x) {
  auto s = __builtin_amdgcn_permlane16_swap(__float_as_uint(x), __float_as_uint(x), false, false);
  x = __uint_as_float(s[0]) + __uint_as_float(s[1]);
  auto t = __builtin_amdgcn_permlane32_swap(__float_as_uint(x), __float_as_uint(x), false, false);
  return __uint_as_float(t[0]) + __uint_as_float(t[1]);
}
__device__ __forceinline__ float x32_sum(float x) {
  auto t = __builtin_amdgcn_permlane32_swap(__float_as_uint(x), __float_as_uint(x), false, false);
  return __uint_as_float(t[0]) + __uint_as_float(t[1]);
}
__device__ __forceinline__ float xrow_max(float x) {
  auto s = __builtin_amdgcn_permlane16_swap(__float_as_uint(x), __float_as_uint(x), false, false);
  x = vmax(__uint_as_float(s[0]), __uint_as_float(s[1]));
  auto t = __builtin_amdgcn_permlane32_swap(__float_as_uint(x), __float_as_uint(x), false, false);
  return vmax(__uint_as_float(t[0]), __uint_as_float(t[1]));
}
__device__ __forceinline__ float wave_sum(float x) {
  x += dpp<0xB1>(x);
  x += dpp<0x4E>(x);
  x += dpp<0x141>(x);
  x += dpp<0x140>(x);
  return xrow_sum(x);
}
__device__ __forceinline__ float sigm(float v) { return __builtin_amdgcn_rcpf(1.f + __builtin_amdgcn_exp2f(-v * LOG2E)); }
__device__ __forceinline__ u32x4 ldmb(__amdgpu_buffer_rsrc_t rs, u32 off) { return __builtin_amdgcn_raw_buffer_load_b128(rs, (int)off, 0, AUX_SC1); }
__device__ __forceinline__ void stmb(__amdgpu_buffer_rsrc_t rs, u32 off, u32x4 v) { __builtin_amdgcn_raw_buffer_store_b128(v, rs, (int)off, 0, AUX_SC1); }
__device__ __forceinline__ u32 tagbad(u32x4 v, u32 tA, u32 tB) { return (v[1] ^ tA) | (v[3] ^ tB); }
template <int N> __device__ __forceinline__ int gather(__amdgpu_buffer_rsrc_t rs, const u32 (&off)[N], u32x4 (&v)[N], u32 tA, u32 tB, bool need) {
  int it = 0;
#pragma unroll 1
  for (; it < SPIN_CAP; ++it) {
    if (__builtin_amdgcn_ballot_w64(need) == 0ull) break;
    asm volatile("" ::: "memory");
    if (need) {
#pragma unroll
      for (int i = 0; i < N; ++i) v[i] = ldmb(rs, off[i]);
      u32 bad = 0;
#pragma unroll
      for (int i = 0; i < N; ++i) bad |= tagbad(v[i], tA, tB);
      need = bad != 0;
    }
  }
  return it;
}
template <int N> __device__ __forceinline__ void nap() { if (N > 0) __builtin_amdgcn_s_sleep(N); }

#define STAMP(k)
#define STAMP_DECL
#define CNT(k, v) (void)(v)
#define STAMP_FLUSH()
}

__global__ void __launch_bounds__(768) fused_decode_kernel(const u16* xp_, const u16* kc_, const u16* vc_, const u16* wq_, const u16* wo_,
                                                           const u16* w1_, const u16* w3_, const u16* w2_, u16* out_, void* ws_) {
  const u16 *xp = xp_, *kc = kc_, *vc = vc_, *wq = wq_, *wo = wo_, *w1 = w1_, *w3 = w3_, *w2 = w2_;
  u16* out = out_;
  void* ws = ws_;
  asm volatile("" : "+s"(xp), "+s"(kc), "+s"(vc), "+s"(wq), "+s"(wo), "+s"(w1), "+s"(w3), "+s"(w2), "+s"(out), "+s"(ws));
  __shared__ __attribute__((aligned(16))) unsigned char smem[L_END];
  LAS unsigned char* L = (LAS unsigned char*)smem;
  const u32 lb = (u32)(uintptr_t)L;
  const int tid = threadIdx.x, lane = tid & 63;
  const int w = __builtin_amdgcn_readfirstlane(tid >> 6);
  const int c = blockIdx.x, x = c & 7, j = c >> 3;
  const bool ffn = j < 28;
  const int n16 = lane & 15, g = lane >> 4;
  const __amdgpu_buffer_rsrc_t rs = __builtin_amdgcn_make_buffer_rsrc(ws, 0, (int)WS_END, 0x00020000);
  const int lg = 7 * x + (j >> 2), mtx = lg & 1, G1 = lg >> 1, kq = j & 3, lgq = j >> 2;
  STAMP_DECL;
  STAMP(0);

  const int wr = (w + j + x) & 7;
  const u32 sw128 = ((((lane & 7) >> 1) ^ g) * 32) + (lane & 1) * 16;
  if (w < 8) {
    const char* sb = (const char*)wq + (size_t)(128 * kq + 16 * wr) * 8192 + (512 * x + 64 * lgq) * 2;
    const u32 vo = (u32)(lane >> 3) * 8192 + sw128;
    dma_w(sb, vo, lb + L_WQ + (16 * wr) * 128);
    dma_w(sb + 8 * 8192, vo, lb + L_WQ + (16 * wr + 8) * 128);
  }

  if (__builtin_expect(w >= 8, COMM_FIRST)) {
    const int cb = w - 8;
    const u32x4 xv = *(const GAS u32x4*)(xp + cb * 512 + lane * 8);
    const u64 E = __hip_atomic_load((GAS u64*)((char*)ws + WS_EPOCH), __ATOMIC_RELAXED, __HIP_MEMORY_SCOPE_AGENT);
    asm volatile("s_barrier" ::: "memory");
    __builtin_amdgcn_s_setprio(3);
    const u32 one = (n16 & 1) ? 0x3F800000u : 0x3F80u;
    const bool lo2 = (n16 & 2) == 0;
    const f32x4 z4 = {0.f, 0.f, 0.f, 0.f};
    {
      *(LAS u32x4*)(L + L_XR + cb * 1024 + lane * 16) = xv;
      float v[8] = {bflo(xv[0]), bfhi(xv[0]), bflo(xv[1]), bfhi(xv[1]), bflo(xv[2]), bfhi(xv[2]), bflo(xv[3]), bfhi(xv[3])};
      float sm = ((v[0] + v[1]) + (v[2] + v[3])) + ((v[4] + v[5]) + (v[6] + v[7]));
      const float mean = wave_sum(sm) * (1.0f / 512.0f);
      float sq = 0.f;
#pragma unroll
      for (int e = 0; e < 8; ++e) { v[e] -= mean; sq = fmaf(v[e], v[e], sq); }
      const float rstd = __builtin_amdgcn_rsqf(wave_sum(sq) * (1.0f / 512.0f) + 1e-5f);
      const u32 o = L_XN + cb * XN_S + (lane >> 2) * 64 + (lane & 1) * 32 + ((lane & 3) >> 1) * 8;
      *(LAS u32x2*)(L + o) = u32x2{pk(v[0] * rstd, v[1] * rstd), pk(v[2] * rstd, v[3] * rstd)};
      *(LAS u32x2*)(L + o + 16) = u32x2{pk(v[4] * rstd, v[5] * rstd), pk(v[6] * rstd, v[7] * rstd)};
    }
    const u64 T = E + 1;
    if (cb == 0 && lane == 0) { const u32 a = (u32)T; *(LAS u32x2*)(L + L_TAG) = u32x2{a, (u32)(E >> 32) ^ ((a << 1) | 1u)}; }
    STAMP(1);
    BAR();
    const u32 tA = *(LAS u32*)(L + L_TAG), tB = *(LAS u32*)(L + L_TAG + 4);
    u32 qo = WS_Q + ((((x * 4 + (j >> 3)) * 4 + cb) * 4) * 32 + lane) * 16;
    u32 go = WS_G + ((((ffn ? j : 0) * 4 + cb) * 2) * 64 + lane) * 16;
    u32 oo = WS_O + (((x * 8 + (j >> 2)) * 32 + 8 * cb + 2 * g) * 24 + n16) * 16;
    u32 so = WS_O + (((x * 8 + (j >> 2)) * 32 + 8 * cb + 2 * g) * 24 + 16 + (n16 & 3)) * 16;
    u32 ho = WS_H + (c * 96 * 2 + lane) * 16;
    u32 a_qw = L_QI + cb * QI_S + lane * 8;
    u32 a_hw = L_HA + cb * OA_S + (n16 >> 3) * 64 + (n16 & 3) * 16 + ((n16 & 7) >> 2) * 8;
    u32 a_ow = L_OA + cb * OA_S + (16 * n16 + 4 * g) * 2;
    const bool qon = (g & 1) == ((n16 >> 2) & 1);
    u32 q01 = (qon && lo2) ? one : 0u, q23 = (qon && !lo2) ? one : 0u;
    const int dw = (n16 & 7) >> 1;
    u32 g0s = dw == 0 ? one : 0u, g1s = dw == 1 ? one : 0u, g2s = dw == 2 ? one : 0u, g3s = dw == 3 ? one : 0u;
    asm volatile("" : "+v"(qo), "+v"(go), "+v"(so), "+v"(oo), "+v"(ho), "+v"(a_qw), "+v"(a_hw), "+v"(a_ow), "+v"(q01), "+v"(q23), "+v"(g0s), "+v"(g1s), "+v"(g2s), "+v"(g3s));
    u32x4 pr0, pr1;
    const u32 oob = 0x7ffffff0u;
    const u32 oq = WS_O + ((x * 8 + (j >> 2)) * 768 + (j & 3) * 192 + lane) * 16;
    const u32 pa0 = cb == 0 ? ((PRER & 1) ? ho : oob) : cb == 1 ? ((PRER & 1) ? ho + 2048 : oob) : cb == 2 ? ((PRER & 2) ? oq : oob) : ((PRER & 2) ? oq + 2048 : oob);
    const u32 pa1 = cb == 0 ? ((PRER & 1) ? ho + 1024 : oob) : cb == 2 ? ((PRER & 2) ? oq + 1024 : oob) : oob;
    pr0 = ldmb(rs, pa0); pr1 = ldmb(rs, pa1);
    STAMP(2);
    BAR();
    BAR();
    STAMP(3);
    {
      nap<D1>();
      const u32 off[2] = {qo, qo + 1024};
      u32x4 v[2];
      CNT(12, gather<2>(rs, off, v, tA, tB, true));
      const f32x4 q = mfma(u32x4{q01, q23, q01, q23}, u32x4{v[0][0], v[0][2], v[1][0], v[1][2]}, z4);
      if (lane < 32) *(LAS u32x2*)(L + a_qw) = u32x2{pk(q.x, q.y), pk(q.z, q.w)};
    }
    asm volatile("" :: "v"(pr0), "v"(pr1));
    STAMP(4);
    BAR();
    BAR();
    u32x4 gv[2];
    if (ffn) { gv[0] = ldmb(rs, go); gv[1] = ldmb(rs, go + 1024); }
    BAR();
    STAMP(5);
    auto hproc = [&]() {
      const f32x4 d = mfma(u32x4{g0s, g1s, g2s, g3s}, u32x4{gv[0][0], gv[0][2], gv[1][0], gv[1][2]}, z4);
      const auto s0 = __builtin_amdgcn_permlane16_swap(__float_as_uint(d.x), __float_as_uint(d.x), false, false);
      const auto s1 = __builtin_amdgcn_permlane16_swap(__float_as_uint(d.y), __float_as_uint(d.y), false, false);
      const auto s2 = __builtin_amdgcn_permlane16_swap(__float_as_uint(d.z), __float_as_uint(d.z), false, false);
      const auto s3 = __builtin_amdgcn_permlane16_swap(__float_as_uint(d.w), __float_as_uint(d.w), false, false);
      const float g0 = __uint_as_float(s0[0]), g1 = __uint_as_float(s1[0]), g2 = __uint_as_float(s2[0]), g3 = __uint_as_float(s3[0]);
      const float u0 = __uint_as_float(s0[1]), u1 = __uint_as_float(s1[1]), u2 = __uint_as_float(s2[1]), u3 = __uint_as_float(s3[1]);
      if (lane < 16) *(LAS u32x2*)(L + a_hw) = u32x2{pk(g0 * sigm(g0) * u0, g1 * sigm(g1) * u1), pk(g2 * sigm(g2) * u2, g3 * sigm(g3) * u3)};
    };
    bool gneed = false, hpend = false;
    if (ffn) {
      gneed = (tagbad(gv[0], tA, tB) | tagbad(gv[1], tA, tB)) != 0;
      hpend = __builtin_amdgcn_ballot_w64(gneed) != 0ull;
      if (!hpend) hproc();
    }
    STAMP(6);
    {
      nap<D2>();
      u32x4 v[4];
      const u32 off[4] = {so, so + 384, oo, oo + 384};
#pragma unroll
      for (int i = 0; i < 4; ++i) v[i] = ldmb(rs, off[i]);
      if (hpend) {
        const u32 goff[2] = {go, go + 1024};
        CNT(13, gather<2>(rs, goff, gv, tA, tB, gneed));
        hproc();
      }
      STAMP(3);
      BAR();
      STAMP(2);
      const bool vneed = (tagbad(v[0], tA, tB) | tagbad(v[1], tA, tB) | tagbad(v[2], tA, tB) | tagbad(v[3], tA, tB)) != 0;
      CNT(14, gather<4>(rs, off, v, tA, tB, vneed));
      STAMP(7);
      const float Ma = __uint_as_float(v[0][0]), La = __uint_as_float(v[0][2]), Mb = __uint_as_float(v[1][0]), Lb = __uint_as_float(v[1][2]);
      const float Mx = xrow_max(vmax(Ma, Mb));
      const float ea = __builtin_amdgcn_exp2f(Ma - Mx), eb = __builtin_amdgcn_exp2f(Mb - Mx);
      const float r = __builtin_amdgcn_rcpf(xrow_sum(fmaf(ea, La, eb * Lb)));
      const u32 wp = pk(ea * r, eb * r);
      const u32 sh = (n16 & 1) * 16;
      const u32 A_ = (wp & 0xffffu) << sh, B_ = (wp >> 16) << sh;
      const u32x4 wf = {lo2 ? A_ : 0u, lo2 ? 0u : A_, lo2 ? B_ : 0u, lo2 ? 0u : B_};
      const f32x4 d = mfma(u32x4{v[2][0], v[2][2], v[3][0], v[3][2]}, wf, z4);
      if (n16 < 4) *(LAS u32x2*)(L + a_ow) = u32x2{pk(d.x, d.y), pk(d.z, d.w)};
    }
    STAMP(8);
    BAR();
    u32 xr = 0;
    if (cb == 0) xr = *(LAS u32*)(L + L_XR + (lane & 3) * 1024 + c * 4);
    BAR();
    if (cb == 0) {
      nap<D3>();
      const bool v2 = lane < 56;
      const u32 off[3] = {ho, ho + 1024, ho + (v2 ? 2048u : 0u)};
      u32x4 h[3];
      CNT(15, gather<3>(rs, off, h, tA, tB, true));
      STAMP(9);
      const u32x4 sel = {lo2 ? one : 0u, lo2 ? 0u : one, lo2 ? one : 0u, lo2 ? 0u : one};
      const f32x4 ds = mfma(u32x4{h[0][0], h[0][2], h[1][0], h[1][2]}, sel, z4);
      const f32x4 df = mfma(u32x4{v2 ? h[2][0] : 0u, v2 ? h[2][2] : 0u, 0u, 0u}, sel, z4);
      const float f0 = xrow_sum(df.x + df.z), f1 = xrow_sum(df.y + df.w), s0 = xrow_sum(ds.x + ds.z), s1 = xrow_sum(ds.y + ds.w);
      if (lane < 4) {
        const float y0 = fmaf(s0, f0 * sigm(f0), bflo(xr)), y1 = fmaf(s1, f1 * sigm(f1), bfhi(xr));
        ((GAS u32*)out)[lane * 256 + c] = pk(y0, y1);
      }
      if (c == 0 && lane == 0) __hip_atomic_store((GAS u64*)((char*)ws + WS_EPOCH), T, __ATOMIC_RELAXED, __HIP_MEMORY_SCOPE_AGENT);
      STAMP(10);
    }
    STAMP_FLUSH();
  } else {
    asm volatile("s_barrier" ::: "memory");
    if (ffn) {
      const char* sb = (const char*)(mtx ? w3 : w1) + (size_t)(128 * kq + 16 * wr) * 3584 + (64 * G1) * 2;
      const u32 vo = (u32)(lane >> 3) * 3584 + sw128;
      dma_w(sb, vo, lb + L_WG + (16 * wr) * 128);
      dma_w(sb + 8 * 3584, vo, lb + L_WG + (16 * wr + 8) * 128);
    }
    const int q4 = n16 >> 2, p4 = lane & 3, t4 = w & 3, h4 = n16 & 3;
    const size_t kvb = ((size_t)(((j >> 3) * 8 + x) * 4096 + 3072 + 128 * (j & 7) + 16 * wr)) * 256;
    const char* kb = (const char*)kc + kvb;
    const char* vb = (const char*)vc + kvb;
    const char* w2b = (const char*)w2 + (size_t)(64 * (ffn ? j : 0) + 8 * wr) * 1024 + x * 128;
    const char* wob = (const char*)wo + (size_t)((4 * x + (wr >> 1)) * 128 + 16 * (j >> 2) + 8 * (wr & 1)) * 1024 + (j & 3) * 256;
    u32 ko[4], vo[4];
#pragma unroll
    for (int i = 0; i < 4; ++i) {
      ko[i] = (4 * i + g) * 256 + ((n16 ^ (4 * i + g)) * 16);
      vo[i] = (4 * i + g) * 256 + ((n16 ^ ((g << 2) | i)) * 16);
    }
    u32 w2o = (u32)(lane >> 3) * 1024 + sw128;
    u32 woo0 = (u32)g * 1024 + ((n16 ^ ((g << 2) | ((2 * wr) & 3))) * 16), woo1 = (u32)(4 + g) * 1024 + ((n16 ^ ((g << 2) | ((2 * wr + 1) & 3))) * 16);
    u32 a_tr = (4 * g + q4) * 128 + (u32)(t4 ^ ((2 * g + (q4 >> 1)) & 3)) * 32 + 8 * p4;
    u32 a_w2 = a_tr + L_W2;
    u32 a_xf = L_XN + h4 * XN_S + kq * 256 + g * 16;
    u32 a_kf[4];
#pragma unroll
    for (int st = 0; st < 4; ++st) a_kf[st] = L_K + (16 * wr + n16) * 256 + (((4 * st + g) ^ n16) * 16);
    u32 a_qf = L_QI + h4 * QI_S + g * 16;
    u32 a_mxw = L_MX + lane * 32 + w * 4, a_mxr = L_MX + h4 * 32;
    u32 a_pw = L_PI + n16 * QI_S + (16 * wr + 4 * g) * 2;
    const u32 ch = 2 * w + (p4 >> 1);
    const u32 ta0 = (8 * g + q4) * 256 + ((ch ^ ((q4 << 2) | ((2 * g) & 3))) * 16) + 8 * (p4 & 1);
    const u32 ta1 = (8 * g + q4 + 4) * 256 + ((ch ^ ((q4 << 2) | ((2 * g + 1) & 3))) * 16) + 8 * (p4 & 1);
    u32 a_v0 = L_V + ta0, a_v1 = L_V + ta1, a_wo0 = L_WO + ta0, a_wo1 = L_WO + ta1;
    u32 a_of = L_OA + h4 * OA_S + g * 16;
    int bsrc = ((lane >> 2) + 16 * (lane & 3)) * 4;
    u32 m_q = w < 4 ? WS_Q + (((((x * 4 + ((lane >> 2) & 3)) * 4 + (lgq >> 1)) * 4 + kq) * 32) + 16 * (lgq & 1) + 4 * t4 + (lane & 3)) * 16
                    : WS_G + ((((G1 * 4 + ((lane >> 2) & 3)) * 2 + mtx) * 4 + kq) * 16 + 4 * t4 + (lane & 3)) * 16;
    u32 m_o = WS_O + (((x * 8 + w) * 32 + j) * 24 + lane) * 16;
    const int colf = 64 * x + 16 * t4 + n16, cols = 128 * (j & 3) + 16 * w + n16;
    u32 m_f = WS_H + (((colf >> 1) * 96 + 64 + j) * 2 + (colf & 1)) * 16;
    u32 m_h = WS_H + (((cols >> 1) * 96 + x * 8 + (j >> 2)) * 2 + (cols & 1)) * 16;
    asm volatile("" : "+v"(ko[0]), "+v"(ko[1]), "+v"(ko[2]), "+v"(ko[3]), "+v"(vo[0]), "+v"(vo[1]), "+v"(vo[2]), "+v"(vo[3]), "+v"(w2o), "+v"(woo0), "+v"(woo1),
                 "+v"(a_tr), "+v"(a_w2), "+v"(a_xf), "+v"(a_kf[0]), "+v"(a_kf[1]), "+v"(a_kf[2]), "+v"(a_kf[3]), "+v"(a_qf), "+v"(a_mxw), "+v"(a_mxr), "+v"(a_pw));
    asm volatile("" : "+v"(a_v0), "+v"(a_v1), "+v"(a_wo0), "+v"(a_wo1), "+v"(a_of), "+v"(bsrc), "+v"(m_q), "+v"(m_o), "+v"(m_f), "+v"(m_h),
                 "+s"(kb), "+s"(vb), "+s"(w2b), "+s"(wob));
    const u32 l_k = lb + L_K + 16 * wr * 256, l_v = lb + L_V + 16 * wr * 256, l_w2 = lb + L_W2 + 8 * wr * 128, l_wo = lb + L_WO + 8 * wr * 256;
    STAMP(1);
    BAR();
    const u32 tA = *(LAS u32*)(L + L_TAG), tB = *(LAS u32*)(L + L_TAG + 4);
    u32x4 xf[4];
#pragma unroll
    for (int st = 0; st < 4; ++st) xf[st] = lds128(L, a_xf + st * 64);
    if (ffn) VMWAIT(2); else VMWAIT(0);
    STAMP(2);
    BAR();
    if (w < 4) {
      u32x4 wf[4];
#pragma unroll
      for (int st = 0; st < 4; ++st) wf[st] = tr2(L, a_tr + L_WQ + st * 4096, a_tr + L_WQ + st * 4096 + 2048);
      __builtin_amdgcn_sched_barrier(0);
      f32x4 acc = {0.f, 0.f, 0.f, 0.f};
#pragma unroll
      for (int st = 0; st < 4; ++st) acc = mfma(wf[st], xf[st], acc);
      const u32 d0 = __builtin_amdgcn_ds_bpermute(bsrc, pk(acc.x, acc.y)), d1 = __builtin_amdgcn_ds_bpermute(bsrc, pk(acc.z, acc.w));
      if (lane < 16) stmb(rs, m_q, u32x4{d0, tA, d1, tB});
    }
    STAMP(3);
#pragma unroll
    for (int i = 0; i < 4; ++i) dma_kv(kb, ko[i], l_k + i * 1024);
    if (w < 4) VMWAIT(5); else VMWAIT(4);
    STAMP(4);
    BAR();
    if (ffn && w >= 4) {
      u32x4 wf[4];
#pragma unroll
      for (int st = 0; st < 4; ++st) wf[st] = tr2(L, a_tr + L_WG + st * 4096, a_tr + L_WG + st * 4096 + 2048);
      __builtin_amdgcn_sched_barrier(0);
      f32x4 acc = {0.f, 0.f, 0.f, 0.f};
#pragma unroll
      for (int st = 0; st < 4; ++st) acc = mfma(wf[st], xf[st], acc);
      const u32 d0 = __builtin_amdgcn_ds_bpermute(bsrc, pk(acc.x, acc.y)), d1 = __builtin_amdgcn_ds_bpermute(bsrc, pk(acc.z, acc.w));
      if (lane < 16) stmb(rs, m_q, u32x4{d0, tA, d1, tB});
    }
#pragma unroll
    for (int i = 0; i < 4; ++i) dma_kv(vb, vo[i], l_v + i * 1024);
    if (ffn && w >= 4) VMWAIT(5); else VMWAIT(4);
    STAMP(5);
    u32x4 kf[4];
#pragma unroll
    for (int st = 0; st < 4; ++st) kf[st] = lds128(L, a_kf[st]);
    BAR();
    STAMP(6);
    f32x4 sc = {0.f, 0.f, 0.f, 0.f};
    {
      u32x4 qf[4];
#pragma unroll
      for (int st = 0; st < 4; ++st) qf[st] = lds128(L, a_qf + st * 64);
      __builtin_amdgcn_sched_barrier(0);
#pragma unroll
      for (int st = 0; st < 4; ++st) sc = mfma(kf[st], qf[st], sc);
    }
    const float mx = xrow_max(vmax(vmax(sc.x, sc.y), vmax(sc.z, sc.w)));
    if (lane < 4) *(LAS float*)(L + a_mxw) = mx;
    BAR();
    float Mc;
    {
      const f32x4 m0 = *(LAS f32x4*)(L + a_mxr), m1 = *(LAS f32x4*)(L + a_mxr + 16);
      Mc = vmax(vmax(vmax(m0.x, m0.y), vmax(m0.z, m0.w)), vmax(vmax(m1.x, m1.y), vmax(m1.z, m1.w))) * SM_C;
      const float p0 = __builtin_amdgcn_exp2f(fmaf(sc.x, SM_C, -Mc)), p1 = __builtin_amdgcn_exp2f(fmaf(sc.y, SM_C, -Mc));
      const float p2 = __builtin_amdgcn_exp2f(fmaf(sc.z, SM_C, -Mc)), p3 = __builtin_amdgcn_exp2f(fmaf(sc.w, SM_C, -Mc));
      if (n16 < 4) *(LAS u32x2*)(L + a_pw) = u32x2{pk(p0, p1), pk(p2, p3)};
    }
    if (ffn) dma_w(w2b, w2o, l_w2);
    dma_w(wob, woo0, l_wo);
    dma_w(wob, woo1, l_wo + 1024);
    if (ffn) VMWAIT(3); else VMWAIT(2);
    STAMP(7);
    BAR();
    {
      u32x4 pf[4], vf[4];
#pragma unroll
      for (int st = 0; st < 4; ++st) { pf[st] = lds128(L, a_qf + 4 * QI_S + st * 64); vf[st] = tr2(L, a_v0 + st * 8192, a_v1 + st * 8192); }
      __builtin_amdgcn_sched_barrier(0);
      f32x4 o = {0.f, 0.f, 0.f, 0.f}, l = {0.f, 0.f, 0.f, 0.f};
      const u32x4 ones = {0x3F803F80u, 0x3F803F80u, 0x3F803F80u, 0x3F803F80u};
#pragma unroll
      for (int st = 0; st < 4; ++st) { o = mfma(pf[st], vf[st], o); l = mfma(pf[st], ones, l); }
      const float l01 = (lane & 1) ? l.y : l.x, l23 = (lane & 1) ? l.w : l.z, lv = (lane & 2) ? l23 : l01;
      const bool dat = lane < 16;
      if (lane < 20) stmb(rs, m_o, u32x4{dat ? pk(o.x, o.y) : __float_as_uint(Mc), tA, dat ? pk(o.z, o.w) : __float_as_uint(lv), tB});
    }
    STAMP(8);
    VMWAIT(1);
    BAR();
    u32x4 wof[2];
#pragma unroll
    for (int st = 0; st < 2; ++st) wof[st] = tr2(L, a_wo0 + st * 8192, a_wo1 + st * 8192);
    if (ffn && w < 4) {
      u32x4 hf[2], w2f[2];
#pragma unroll
      for (int st = 0; st < 2; ++st) { hf[st] = lds128(L, a_of + 4 * OA_S + st * 64); w2f[st] = tr2(L, a_w2 + st * 4096, a_w2 + st * 4096 + 2048); }
      __builtin_amdgcn_sched_barrier(0);
      f32x4 f = {0.f, 0.f, 0.f, 0.f};
#pragma unroll
      for (int st = 0; st < 2; ++st) f = mfma(hf[st], w2f[st], f);
      if (lane < 16) stmb(rs, m_f, u32x4{pk(f.x, f.y), tA, pk(f.z, f.w), tB});
    }
    asm volatile("" : "+v"(wof[0]), "+v"(wof[1]));
    STAMP(9);
    BAR();
    STAMP(10);
    {
      const u32x4 of0 = lds128(L, a_of), of1 = lds128(L, a_of + 64);
      f32x4 a = {0.f, 0.f, 0.f, 0.f};
      a = mfma(of0, wof[0], a);
      a = mfma(of1, wof[1], a);
      if (lane < 16) stmb(rs, m_h, u32x4{pk(a.x, a.y), tA, pk(a.z, a.w), tB});
    }
    STAMP(11);
    BAR();
    STAMP_FLUSH();
  }
}

extern "C" void kernel_launch(void* const* d_in, const int* in_sizes, int n_in, void* d_out, int out_size, void* d_ws, size_t ws_size,
                              hipStream_t stream) {
  fused_decode_kernel<<<256, 768, 0, stream>>>((const u16*)d_in[0], (const u16*)d_in[1], (const u16*)d_in[2], (const u16*)d_in[3],
                                               (const u16*)d_in[4], (const u16*)d_in[6], (const u16*)d_in[7], (const u16*)d_in[8],
                                               (u16*)d_out, d_ws);
}
```

```cpp
#include <hip/hip_runtime.h>
#include <stdint.h>

namespace {
typedef unsigned short u16;
typedef unsigned u32;
typedef unsigned long long u64;
typedef u32 u32x4 __attribute__((ext_vector_type(4)));
typedef u32 u32x2 __attribute__((ext_vector_type(2)));
typedef float f32x4 __attribute__((ext_vector_type(4)));
typedef float f32x2 __attribute__((ext_vector_type(2)));
typedef short s16x4 __attribute__((ext_vector_type(4)));
typedef __bf16 bf16x2 __attribute__((ext_vector_type(2)));
typedef __bf16 bf16x8 __attribute__((ext_vector_type(8)));
#define LAS __attribute__((address_space(3)))
#define GAS __attribute__((address_space(1)))

constexpr u32 WS_EPOCH = 0;
constexpr u32 WS_Q = 4096;
constexpr u32 WS_G = WS_Q + 262144;
constexpr u32 WS_O = WS_G + 229376;
constexpr u32 WS_H = WS_O + 786432;
constexpr u32 WS_END = WS_H + 92 * 512 * 16;
constexpr u32 OOB = 0x7ffffff0u;

constexpr u32 L_WQ = 0, L_WG = 16384, L_K = 32768, L_V = 65536, L_WO = 98304, L_W2 = 114688;
constexpr u32 L_XN = 122880, XN_S = 1088;
constexpr u32 L_XR = L_XN + 4 * XN_S;
constexpr u32 L_QI = L_XR + 4096, QI_S = 320;
constexpr u32 L_PI = L_QI + 4 * QI_S;
constexpr u32 L_MX = L_PI + 4 * QI_S;
constexpr u32 L_OA = L_MX + 128, OA_S = 192;
constexpr u32 L_HA = L_OA + 4 * OA_S;
constexpr u32 L_TAG = L_HA + 4 * OA_S;
constexpr u32 L_END = L_TAG + 16;

constexpr float LOG2E = 1.4426950408889634f;
constexpr float SM_C = 0.08838834764831845f * LOG2E;
constexpr int SPIN_CAP = 1 << 16;

constexpr int NAP_Q = 10, NAP_O = 30, NAP_S = 24;

__device__ __forceinline__ void dma(const void* base, u32 voff, u32 lds) {
  asm volatile("s_mov_b32 m0, %2\n\ts_nop 4\n\tglobal_load_lds_dwordx4 %0, %1 nt" ::"v"(voff), "s"(base), "s"(lds) : "memory");
}
#define VMWAIT(n) asm volatile("s_waitcnt vmcnt(" #n ")" ::: "memory")
#define BAR() asm volatile("s_waitcnt lgkmcnt(0)\n\ts_barrier" ::: "memory")
#define PIN_V(...) asm volatile("" : __VA_ARGS__)

__device__ __forceinline__ u32 pk(float lo, float hi) {
  f32x2 v = {lo, hi};
  return __builtin_bit_cast(u32, __builtin_convertvector(v, bf16x2));
}
__device__ __forceinline__ float bflo(u32 v) { return __uint_as_float(v << 16); }
__device__ __forceinline__ float bfhi(u32 v) { return __uint_as_float(v & 0xffff0000u); }
__device__ __forceinline__ f32x4 mfma(u32x4 a, u32x4 b, f32x4 c) {
  return __builtin_amdgcn_mfma_f32_16x16x32_bf16(__builtin_bit_cast(bf16x8, a), __builtin_bit_cast(bf16x8, b), c, 0, 0, 0);
}
__device__ __forceinline__ u32x4 lds128(LAS unsigned char* L, u32 off) { return *(LAS u32x4*)(L + off); }
__device__ __forceinline__ u32x4 tr2(LAS unsigned char* L, u32 off0, u32 off1) {
  u32x2 a = __builtin_bit_cast(u32x2, __builtin_amdgcn_ds_read_tr16_b64_v4i16((LAS s16x4*)(L + off0)));
  u32x2 b = __builtin_bit_cast(u32x2, __builtin_amdgcn_ds_read_tr16_b64_v4i16((LAS s16x4*)(L + off1)));
  return u32x4{a.x, a.y, b.x, b.y};
}
template <int CTRL> __device__ __forceinline__ float dpp(float x) {
  return __builtin_bit_cast(float, __builtin_amdgcn_mov_dpp(__builtin_bit_cast(int, x), CTRL, 0xf, 0xf, true));
}
__device__ __forceinline__ float pinf() { float r; asm("v_mov_b32 %0, 0x7f800000" : "=v"(r)); return r; }
__device__ __forceinline__ float vmax(float a, float b) { return __builtin_amdgcn_fmed3f(a, b, pinf()); }
__device__ __forceinline__ float xrow_sum(float x) {
  auto s = __builtin_amdgcn_permlane16_swap(__float_as_uint(x), __float_as_uint(x), false, false);
  x = __uint_as_float(s[0]) + __uint_as_float(s[1]);
  auto t = __builtin_amdgcn_permlane32_swap(__float_as_uint(x), __float_as_uint(x), false, false);
  return __uint_as_float(t[0]) + __uint_as_float(t[1]);
}
__device__ __forceinline__ f32x4 xrow_sum4(f32x4 x) {
  const auto s0 = __builtin_amdgcn_permlane16_swap(__float_as_uint(x.x), __float_as_uint(x.x), false, false);
  const auto s1 = __builtin_amdgcn_permlane16_swap(__float_as_uint(x.y), __float_as_uint(x.y), false, false);
  const auto s2 = __builtin_amdgcn_permlane16_swap(__float_as_uint(x.z), __float_as_uint(x.z), false, false);
  const auto s3 = __builtin_amdgcn_permlane16_swap(__float_as_uint(x.w), __float_as_uint(x.w), false, false);
  __builtin_amdgcn_sched_barrier(0);
  const u32 a = __float_as_uint(__uint_as_float(s0[0]) + __uint_as_float(s0[1])), b = __float_as_uint(__uint_as_float(s1[0]) + __uint_as_float(s1[1]));
  const u32 c = __float_as_uint(__uint_as_float(s2[0]) + __uint_as_float(s2[1])), d = __float_as_uint(__uint_as_float(s3[0]) + __uint_as_float(s3[1]));
  const auto t0 = __builtin_amdgcn_permlane32_swap(a, a, false, false), t1 = __builtin_amdgcn_permlane32_swap(b, b, false, false);
  const auto t2 = __builtin_amdgcn_permlane32_swap(c, c, false, false), t3 = __builtin_amdgcn_permlane32_swap(d, d, false, false);
  return f32x4{__uint_as_float(t0[0]) + __uint_as_float(t0[1]), __uint_as_float(t1[0]) + __uint_as_float(t1[1]),
               __uint_as_float(t2[0]) + __uint_as_float(t2[1]), __uint_as_float(t3[0]) + __uint_as_float(t3[1])};
}
__device__ __forceinline__ float xrow_max(float x) {
  auto s = __builtin_amdgcn_permlane16_swap(__float_as_uint(x), __float_as_uint(x), false, false);
  x = vmax(__uint_as_float(s[0]), __uint_as_float(s[1]));
  auto t = __builtin_amdgcn_permlane32_swap(__float_as_uint(x), __float_as_uint(x), false, false);
  return vmax(__uint_as_float(t[0]), __uint_as_float(t[1]));
}
__device__ __forceinline__ float wave_sum(float x) {
  x += dpp<0xB1>(x);
  x += dpp<0x4E>(x);
  x += dpp<0x141>(x);
  x += dpp<0x140>(x);
  return xrow_sum(x);
}
__device__ __forceinline__ float sigm(float v) { return __builtin_amdgcn_rcpf(1.f + __builtin_amdgcn_exp2f(-v * LOG2E)); }

__device__ __forceinline__ u32x4 ldmb(__amdgpu_buffer_rsrc_t rs, u32 off) { return __builtin_amdgcn_raw_buffer_load_b128(rs, (int)off, 0, 16); }
__device__ __forceinline__ void stmb(__amdgpu_buffer_rsrc_t rs, u32 off, u32x4 v) { __builtin_amdgcn_raw_buffer_store_b128(v, rs, (int)off, 0, 16); }
template <int N> __device__ __forceinline__ bool missing(const u32x4 (&v)[N], u32 tA, u32 tB) {
  u32 bad = 0;
#pragma unroll
  for (int i = 0; i < N; ++i) bad |= (v[i][1] ^ tA) | (v[i][3] ^ tB);
  return bad != 0;
}
template <int N> __device__ __forceinline__ void request(__amdgpu_buffer_rsrc_t rs, const u32 (&off)[N], u32x4 (&v)[N]) {
#pragma unroll
  for (int i = 0; i < N; ++i) v[i] = ldmb(rs, off[i]);
}
template <int N> __device__ __forceinline__ void complete(__amdgpu_buffer_rsrc_t rs, const u32 (&off)[N], u32x4 (&v)[N], u32 tA, u32 tB) {
  bool need = missing<N>(v, tA, tB);
  if (__builtin_expect(__builtin_amdgcn_ballot_w64(need) != 0ull, 1)) {
    int it = 0;
#pragma unroll 1
    do {
      asm volatile("" ::: "memory");
      if (need) { request<N>(rs, off, v); need = missing<N>(v, tA, tB); }
    } while (__builtin_amdgcn_ballot_w64(need) != 0ull && ++it < SPIN_CAP);
  }
}
template <int N> __device__ __forceinline__ void nap() { if (N > 0) __builtin_amdgcn_s_sleep(N); }

}

__global__ void __launch_bounds__(768, 3) fused_decode_kernel(const u16* xp_, const u16* kc_, const u16* vc_, const u16* wq_, const u16* wo_,
                                                              const u16* w1_, const u16* w3_, const u16* w2_, u16* out_, void* ws_) {
  const u16 *xp = xp_, *kc = kc_, *vc = vc_, *wq = wq_, *wo = wo_, *w1 = w1_, *w3 = w3_, *w2 = w2_;
  u16* out = out_;
  void* ws = ws_;
  asm volatile("" : "+s"(xp), "+s"(kc), "+s"(vc), "+s"(wq), "+s"(wo), "+s"(w1), "+s"(w3), "+s"(w2), "+s"(out), "+s"(ws));
  __shared__ __attribute__((aligned(16))) unsigned char smem[L_END];
  LAS unsigned char* L = (LAS unsigned char*)smem;
  const u32 lb = (u32)(uintptr_t)L;
  const int lane = threadIdx.x & 63;
  const int wv = __builtin_amdgcn_readfirstlane(threadIdx.x >> 6);
  const int w = wv < 4 ? wv + 8 : wv - 4;
  const int c = blockIdx.x, x = c & 7, j = c >> 3;
  const bool ffn = j < 28;
  const int n16 = lane & 15, g = lane >> 4;
  const __amdgpu_buffer_rsrc_t rs = __builtin_amdgcn_make_buffer_rsrc(ws, 0, (int)WS_END, 0x00020000);
  const int kq = j & 3, lgq = j >> 2;
  const int lg = 7 * x + lgq, mtx = lg & 1, G1 = lg >> 1;

  const int wr = (w + j + x) & 7;
  const u32 sw128 = ((((lane & 7) >> 1) ^ g) * 32) + (lane & 1) * 16;
  if (w < 8) {
    const char* sb = (const char*)wq + (size_t)(128 * kq + 16 * wr) * 8192 + (512 * x + 64 * lgq) * 2;
    const u32 vo = (u32)(lane >> 3) * 8192 + sw128;
    dma(sb, vo, lb + L_WQ + (16 * wr) * 128);
    dma(sb + 8 * 8192, vo, lb + L_WQ + (16 * wr + 8) * 128);
  }

  if (__builtin_expect(w >= 8, 1)) {
    const int cb = w - 8;
    const u32x4 xv = *(const GAS u32x4*)(xp + cb * 512 + lane * 8);
    const u64 E = __hip_atomic_load((GAS u64*)((char*)ws + WS_EPOCH), __ATOMIC_RELAXED, __HIP_MEMORY_SCOPE_AGENT);
    asm volatile("s_barrier" ::: "memory");
    __builtin_amdgcn_s_setprio(3);
    {
      *(LAS u32x4*)(L + L_XR + cb * 1024 + lane * 16) = xv;
      float v[8] = {bflo(xv[0]), bfhi(xv[0]), bflo(xv[1]), bfhi(xv[1]), bflo(xv[2]), bfhi(xv[2]), bflo(xv[3]), bfhi(xv[3])};
      const float sm = ((v[0] + v[1]) + (v[2] + v[3])) + ((v[4] + v[5]) + (v[6] + v[7]));
      const float mean = wave_sum(sm) * (1.0f / 512.0f);
      float sq = 0.f;
#pragma unroll
      for (int e = 0; e < 8; ++e) { v[e] -= mean; sq = fmaf(v[e], v[e], sq); }
      const float rstd = __builtin_amdgcn_rsqf(wave_sum(sq) * (1.0f / 512.0f) + 1e-5f);
      const u32 o = L_XN + cb * XN_S + (lane >> 2) * 64 + (lane & 1) * 32 + ((lane & 3) >> 1) * 8;
      *(LAS u32x2*)(L + o) = u32x2{pk(v[0] * rstd, v[1] * rstd), pk(v[2] * rstd, v[3] * rstd)};
      *(LAS u32x2*)(L + o + 16) = u32x2{pk(v[4] * rstd, v[5] * rstd), pk(v[6] * rstd, v[7] * rstd)};
    }
    const u64 T = E + 1;
    if (cb == 0 && lane == 0) { const u32 a = (u32)T; *(LAS u32x2*)(L + L_TAG) = u32x2{a, (u32)(E >> 32) ^ ((a << 1) | 1u)}; }
    BAR();
    const u32 tA = *(LAS u32*)(L + L_TAG), tB = *(LAS u32*)(L + L_TAG + 4);
    const int dt = j >> 2, fc = x * 32 + j;
    u32 qo = WS_Q + ((((x * 4 + (j >> 3)) * 4 + cb) * 4) * 32 + lane) * 16;
    u32 go = WS_G + ((((ffn ? j : 0) * 4 + cb) * 2) * 64 + lane) * 16;
    u32 oo = WS_O + (((x * 8 + dt) * 32 + 8 * cb + 2 * g) * 24 + n16) * 16;
    u32 so = WS_O + (((x * 8 + dt) * 32 + 8 * cb + 2 * g) * 24 + 16 + (n16 & 3)) * 16;
    u32 ho = WS_H + ((lane >> 1) * 512 + 2 * fc + (lane & 1)) * 16;
    u32 ho2 = lane < 56 ? ho + 64 * 512 * 16 : ho;
    u32 a_qw = L_QI + cb * QI_S + lane * 8;
    u32 a_hw = L_HA + cb * OA_S + (n16 >> 3) * 64 + (n16 & 3) * 16 + ((n16 & 7) >> 2) * 8;
    u32 a_ow = L_OA + cb * OA_S + (16 * n16 + 4 * g) * 2;
    const u32 one = (n16 & 1) ? 0x3F800000u : 0x3F80u;
    const bool lo2 = (n16 & 2) == 0, qon = (g & 1) == ((n16 >> 2) & 1);
    u32 c01 = lo2 ? one : 0u, c23 = lo2 ? 0u : one;
    u32 q01 = qon ? c01 : 0u, q23 = qon ? c23 : 0u;
    const int dw = (n16 & 7) >> 1;
    u32 g0s = dw == 0 ? one : 0u, g1s = dw == 1 ? one : 0u, g2s = dw == 2 ? one : 0u, g3s = dw == 3 ? one : 0u;
    PIN_V("+v"(qo), "+v"(go), "+v"(so), "+v"(oo), "+v"(ho), "+v"(ho2), "+v"(a_qw), "+v"(a_hw), "+v"(a_ow), "+v"(c01), "+v"(c23), "+v"(q01), "+v"(q23),
          "+v"(g0s), "+v"(g1s), "+v"(g2s), "+v"(g3s));
    const f32x4 z4 = {0.f, 0.f, 0.f, 0.f};
    const u32 oq = WS_O + ((x * 8 + dt) * 768 + (j & 3) * 192 + lane) * 16;
    const u32x4 pr0 = ldmb(rs, cb == 2 ? oq : cb == 3 ? oq + 2048 : OOB), pr1 = ldmb(rs, cb == 2 ? oq + 1024 : OOB);
    BAR();
    BAR();
    {
      nap<NAP_Q>();
      const u32 off[2] = {qo, qo + 1024};
      u32x4 v[2];
      request<2>(rs, off, v);
      complete<2>(rs, off, v, tA, tB);
      const f32x4 q = mfma(u32x4{q01, q23, q01, q23}, u32x4{v[0][0], v[0][2], v[1][0], v[1][2]}, z4);
      if (lane < 32) *(LAS u32x2*)(L + a_qw) = u32x2{pk(q.x, q.y), pk(q.z, q.w)};
    }
    asm volatile("" ::"v"(pr0), "v"(pr1));
    BAR();
    BAR();
    const u32 goff[2] = {go, go + 1024};
    u32x4 gv[2];
    if (ffn) request<2>(rs, goff, gv);
    BAR();
    auto make_h = [&]() {
      const f32x4 d = mfma(u32x4{g0s, g1s, g2s, g3s}, u32x4{gv[0][0], gv[0][2], gv[1][0], gv[1][2]}, z4);
      const auto s0 = __builtin_amdgcn_permlane16_swap(__float_as_uint(d.x), __float_as_uint(d.x), false, false);
      const auto s1 = __builtin_amdgcn_permlane16_swap(__float_as_uint(d.y), __float_as_uint(d.y), false, false);
      const auto s2 = __builtin_amdgcn_permlane16_swap(__float_as_uint(d.z), __float_as_uint(d.z), false, false);
      const auto s3 = __builtin_amdgcn_permlane16_swap(__float_as_uint(d.w), __float_as_uint(d.w), false, false);
      const float g0 = __uint_as_float(s0[0]), g1 = __uint_as_float(s1[0]), g2 = __uint_as_float(s2[0]), g3 = __uint_as_float(s3[0]);
      const float u0 = __uint_as_float(s0[1]), u1 = __uint_as_float(s1[1]), u2 = __uint_as_float(s2[1]), u3 = __uint_as_float(s3[1]);
      if (lane < 16) *(LAS u32x2*)(L + a_hw) = u32x2{pk(g0 * sigm(g0) * u0, g1 * sigm(g1) * u1), pk(g2 * sigm(g2) * u2, g3 * sigm(g3) * u3)};
    };
    bool hpend = false;
    if (ffn) {
      hpend = __builtin_amdgcn_ballot_w64(missing<2>(gv, tA, tB)) != 0ull;
      if (!hpend) make_h();
    }
    {
      nap<NAP_O>();
      const u32 off[4] = {so, so + 384, oo, oo + 384};
      u32x4 v[4];
      request<4>(rs, off, v);
      if (hpend) {
        complete<2>(rs, goff, gv, tA, tB);
        make_h();
      }
      u32 tA7 = tA, tB7 = tB;
      asm volatile("s_waitcnt lgkmcnt(0)\n\ts_barrier" : "+v"(tA7), "+v"(tB7)::"memory");
      complete<4>(rs, off, v, tA7, tB7);
      const float Ma = __uint_as_float(v[0][0]), La = __uint_as_float(v[0][2]), Mb = __uint_as_float(v[1][0]), Lb = __uint_as_float(v[1][2]);
      const float Mx = xrow_max(vmax(Ma, Mb));
      const float ea = __builtin_amdgcn_exp2f(Ma - Mx), eb = __builtin_amdgcn_exp2f(Mb - Mx);
      const float r = __builtin_amdgcn_rcpf(xrow_sum(fmaf(ea, La, eb * Lb)));
      const u32 wp = pk(ea * r, eb * r);
      const u32 sh = (n16 & 1) * 16;
      const u32 wa = (wp & 0xffffu) << sh, wb = (wp >> 16) << sh;
      const f32x4 d = mfma(u32x4{v[2][0], v[2][2], v[3][0], v[3][2]}, u32x4{lo2 ? wa : 0u, lo2 ? 0u : wa, lo2 ? wb : 0u, lo2 ? 0u : wb}, z4);
      if (n16 < 4) *(LAS u32x2*)(L + a_ow) = u32x2{pk(d.x, d.y), pk(d.z, d.w)};
    }
    BAR();
    u32 xr = *(LAS u32*)(L + L_XR + (lane & 3) * 1024 + fc * 4);
    GAS u32* op = (GAS u32*)out + (lane & 3) * 256 + fc;
    u32 m2 = lane < 56 ? 0xffffffffu : 0u;
    PIN_V("+v"(xr), "+v"(op), "+v"(m2));
    BAR();
    if (cb == 0) {
      nap<NAP_S>();
      const u32 off[3] = {ho, ho + 32 * 512 * 16, ho2};
      u32x4 h[3];
      request<3>(rs, off, h);
      complete<3>(rs, off, h, tA, tB);
      if (c == 0 && lane == 0) __hip_atomic_store((GAS u64*)((char*)ws + WS_EPOCH), T, __ATOMIC_RELAXED, __HIP_MEMORY_SCOPE_AGENT);
      const u32x4 sel = {c01, c23, c01, c23};
      const f32x4 df = mfma(u32x4{h[2][0] & m2, h[2][2] & m2, 0u, 0u}, sel, z4);
      const f32x4 ds = mfma(u32x4{h[0][0], h[0][2], h[1][0], h[1][2]}, sel, z4);
      const f32x4 t = xrow_sum4(f32x4{df.x + df.z, df.y + df.w, ds.x + ds.z, ds.y + ds.w});
      const float y0 = fmaf(t.z, t.x * sigm(t.x), bflo(xr)), y1 = fmaf(t.w, t.y * sigm(t.y), bfhi(xr));
      if (lane < 4) *op = pk(y0, y1);
    }
  } else {
    asm volatile("s_barrier" ::: "memory");
    if (ffn) {
      const char* sb = (const char*)(mtx ? w3 : w1) + (size_t)(128 * kq + 16 * wr) * 3584 + (64 * G1) * 2;
      const u32 vo = (u32)(lane >> 3) * 3584 + sw128;
      dma(sb, vo, lb + L_WG + (16 * wr) * 128);
      dma(sb + 8 * 3584, vo, lb + L_WG + (16 * wr + 8) * 128);
    }
    const int t4 = w & 3;
    const size_t kvb = ((size_t)(((j >> 3) * 8 + x) * 4096 + 3072 + 128 * (j & 7) + 16 * wr)) * 256;
    const char* kb = (const char*)kc + kvb;
    const char* vb = (const char*)vc + kvb;
    u32 ko[4], vo[4];
#pragma unroll
    for (int i = 0; i < 4; ++i) {
      ko[i] = (4 * i + g) * 256 + ((n16 ^ (4 * i + g)) * 16);
      vo[i] = (4 * i + g) * 256 + ((n16 ^ ((g << 2) | i)) * 16);
    }
    u32 a_tr = (4 * g + (n16 >> 2)) * 128 + (u32)(t4 ^ ((2 * g + (n16 >> 3)) & 3)) * 32 + 8 * (lane & 3);
    u32 a_xf = L_XN + (n16 & 3) * XN_S + kq * 256 + g * 16;
    int bsrc = ((lane >> 2) + 16 * (lane & 3)) * 4;
    u32 m_q = w < 4 ? WS_Q + (((((x * 4 + ((lane >> 2) & 3)) * 4 + (lgq >> 1)) * 4 + kq) * 32) + 16 * (lgq & 1) + 4 * t4 + (lane & 3)) * 16
                    : WS_G + ((((G1 * 4 + ((lane >> 2) & 3)) * 2 + mtx) * 4 + kq) * 16 + 4 * t4 + (lane & 3)) * 16;
    PIN_V("+v"(ko[0]), "+v"(ko[1]), "+v"(ko[2]), "+v"(ko[3]), "+v"(vo[0]), "+v"(vo[1]), "+v"(vo[2]), "+v"(vo[3]), "+v"(a_tr), "+v"(a_xf), "+v"(bsrc),
          "+v"(m_q), "+s"(kb), "+s"(vb));
    const u32 l_k = lb + L_K + 16 * wr * 256, l_v = lb + L_V + 16 * wr * 256, l_w2 = lb + L_W2 + 8 * wr * 128, l_wo = lb + L_WO + 8 * wr * 256;
    BAR();
    const u32 tA = *(LAS u32*)(L + L_TAG), tB = *(LAS u32*)(L + L_TAG + 4);
    u32x4 xf[4];
#pragma unroll
    for (int st = 0; st < 4; ++st) xf[st] = lds128(L, a_xf + st * 64);
    auto gemv_tile = [&](u32 l_w) {
      u32x4 wf[4];
#pragma unroll
      for (int st = 0; st < 4; ++st) wf[st] = tr2(L, a_tr + l_w + st * 4096, a_tr + l_w + st * 4096 + 2048);
      __builtin_amdgcn_sched_barrier(0);
      f32x4 acc = {0.f, 0.f, 0.f, 0.f};
#pragma unroll
      for (int st = 0; st < 4; ++st) acc = mfma(wf[st], xf[st], acc);
      const u32 d0 = __builtin_amdgcn_ds_bpermute(bsrc, pk(acc.x, acc.y)), d1 = __builtin_amdgcn_ds_bpermute(bsrc, pk(acc.z, acc.w));
      if (lane < 16) stmb(rs, m_q, u32x4{d0, tA, d1, tB});
    };
    if (ffn) VMWAIT(2); else VMWAIT(0);
    BAR();
    if (w < 4) gemv_tile(L_WQ);
#pragma unroll
    for (int i = 0; i < 4; ++i) dma(kb, ko[i], l_k + i * 1024);
    if (w < 4) VMWAIT(5); else VMWAIT(4);
    BAR();
    if (ffn && w >= 4) gemv_tile(L_WG);
#pragma unroll
    for (int i = 0; i < 4; ++i) dma(vb, vo[i], l_v + i * 1024);
    int ln = lane;
    PIN_V("+v"(ln));
    const int n16b = ln & 15, gb = ln >> 4, q4b = n16b >> 2, p4b = ln & 3, h4b = n16b & 3;
    const char* w2b = (const char*)w2 + (size_t)(64 * (ffn ? j : 0) + 8 * wr) * 1024 + x * 128;
    const char* wob = (const char*)wo + (size_t)((4 * x + (wr >> 1)) * 128 + 16 * (j >> 2) + 8 * (wr & 1)) * 1024 + (j & 3) * 256;
    u32 w2o = (u32)(ln >> 3) * 1024 + ((((ln & 7) >> 1) ^ gb) * 32) + (ln & 1) * 16;
    u32 woo0 = (u32)gb * 1024 + ((n16b ^ ((gb << 2) | ((2 * wr) & 3))) * 16), woo1 = (u32)(4 + gb) * 1024 + ((n16b ^ ((gb << 2) | ((2 * wr + 1) & 3))) * 16);
    u32 a_w2 = a_tr + L_W2;
    u32 a_kf[4];
#pragma unroll
    for (int st = 0; st < 4; ++st) a_kf[st] = L_K + (16 * wr + n16b) * 256 + (((4 * st + gb) ^ n16b) * 16);
    u32 a_qf = L_QI + h4b * QI_S + gb * 16;
    u32 a_mxw = L_MX + ln * 32 + w * 4, a_mxr = L_MX + h4b * 32;
    u32 a_pw = L_PI + n16b * QI_S + (16 * wr + 4 * gb) * 2;
    const u32 ch = 2 * w + (p4b >> 1);
    const u32 ta0 = (8 * gb + q4b) * 256 + ((ch ^ ((q4b << 2) | ((2 * gb) & 3))) * 16) + 8 * (p4b & 1);
    const u32 ta1 = (8 * gb + q4b + 4) * 256 + ((ch ^ ((q4b << 2) | ((2 * gb + 1) & 3))) * 16) + 8 * (p4b & 1);
    u32 a_v0 = L_V + ta0, a_v1 = L_V + ta1, a_wo0 = L_WO + ta0, a_wo1 = L_WO + ta1;
    u32 a_of = L_OA + h4b * OA_S + gb * 16;
    u32 m_o = WS_O + (((x * 8 + w) * 32 + j) * 24 + ln) * 16;
    u32 m_f = WS_H + ((64 + j) * 512 + 64 * x + 16 * t4 + n16b) * 16;
    u32 m_h = WS_H + ((x * 8 + (j >> 2)) * 512 + 128 * (j & 3) + 16 * w + n16b) * 16;
    PIN_V("+v"(w2o), "+v"(woo0), "+v"(woo1), "+v"(a_w2), "+v"(a_kf[0]), "+v"(a_kf[1]), "+v"(a_kf[2]), "+v"(a_kf[3]), "+v"(a_qf), "+v"(a_mxw), "+v"(a_mxr),
          "+v"(a_pw), "+v"(a_v0), "+v"(a_v1), "+v"(a_wo0), "+v"(a_wo1), "+v"(a_of), "+v"(m_o), "+v"(m_f), "+v"(m_h), "+s"(w2b), "+s"(wob));
    if (ffn && w >= 4) VMWAIT(5); else VMWAIT(4);
    u32x4 kf[4];
#pragma unroll
    for (int st = 0; st < 4; ++st) kf[st] = lds128(L, a_kf[st]);
    BAR();
    f32x4 sc = {0.f, 0.f, 0.f, 0.f};
    {
      u32x4 qf[4];
#pragma unroll
      for (int st = 0; st < 4; ++st) qf[st] = lds128(L, a_qf + st * 64);
      __builtin_amdgcn_sched_barrier(0);
#pragma unroll
      for (int st = 0; st < 4; ++st) sc = mfma(kf[st], qf[st], sc);
    }
    const float mx = xrow_max(vmax(vmax(sc.x, sc.y), vmax(sc.z, sc.w)));
    if (lane < 4) *(LAS float*)(L + a_mxw) = mx;
    BAR();
    float Mc;
    {
      const f32x4 m0 = *(LAS f32x4*)(L + a_mxr), m1 = *(LAS f32x4*)(L + a_mxr + 16);
      Mc = vmax(vmax(vmax(m0.x, m0.y), vmax(m0.z, m0.w)), vmax(vmax(m1.x, m1.y), vmax(m1.z, m1.w))) * SM_C;
      const float p0 = __builtin_amdgcn_exp2f(fmaf(sc.x, SM_C, -Mc)), p1 = __builtin_amdgcn_exp2f(fmaf(sc.y, SM_C, -Mc));
      const float p2 = __builtin_amdgcn_exp2f(fmaf(sc.z, SM_C, -Mc)), p3 = __builtin_amdgcn_exp2f(fmaf(sc.w, SM_C, -Mc));
      if (n16 < 4) *(LAS u32x2*)(L + a_pw) = u32x2{pk(p0, p1), pk(p2, p3)};
    }
    if (ffn) dma(w2b, w2o, l_w2);
    dma(wob, woo0, l_wo);
    dma(wob, woo1, l_wo + 1024);
    if (ffn) VMWAIT(3); else VMWAIT(2);
    BAR();
    {
      u32x4 pf[4], vf[4];
#pragma unroll
      for (int st = 0; st < 4; ++st) { pf[st] = lds128(L, a_qf + 4 * QI_S + st * 64); vf[st] = tr2(L, a_v0 + st * 8192, a_v1 + st * 8192); }
      __builtin_amdgcn_sched_barrier(0);
      f32x4 o = {0.f, 0.f, 0.f, 0.f}, l = {0.f, 0.f, 0.f, 0.f};
      const u32x4 ones = {0x3F803F80u, 0x3F803F80u, 0x3F803F80u, 0x3F803F80u};
#pragma unroll
      for (int st = 0; st < 4; ++st) { o = mfma(pf[st], vf[st], o); l = mfma(pf[st], ones, l); }
      u32 d0 = pk(o.x, o.y), d1 = pk(o.z, o.w);
      float l01 = (lane & 1) ? l.y : l.x, l23 = (lane & 1) ? l.w : l.z;
      PIN_V("+v"(d0), "+v"(d1), "+v"(l01), "+v"(l23));
      const float lv = (lane & 2) ? l23 : l01;
      const bool dat = lane < 16;
      d0 = dat ? d0 : __float_as_uint(Mc);
      d1 = dat ? d1 : __float_as_uint(lv);
      PIN_V("+v"(d0), "+v"(d1));
      if (lane < 20) stmb(rs, m_o, u32x4{d0, tA, d1, tB});
    }
    VMWAIT(1);
    BAR();
    u32x4 wof[2];
#pragma unroll
    for (int st = 0; st < 2; ++st) wof[st] = tr2(L, a_wo0 + st * 8192, a_wo1 + st * 8192);
    if (ffn && w < 4) {
      u32x4 hf[2], w2f[2];
#pragma unroll
      for (int st = 0; st < 2; ++st) { hf[st] = lds128(L, a_of + 4 * OA_S + st * 64); w2f[st] = tr2(L, a_w2 + st * 4096, a_w2 + st * 4096 + 2048); }
      __builtin_amdgcn_sched_barrier(0);
      f32x4 f = {0.f, 0.f, 0.f, 0.f};
#pragma unroll
      for (int st = 0; st < 2; ++st) f = mfma(hf[st], w2f[st], f);
      if (lane < 16) stmb(rs, m_f, u32x4{pk(f.x, f.y), tA, pk(f.z, f.w), tB});
    }
    PIN_V("+v"(wof[0]), "+v"(wof[1]));
    BAR();
    {
      const u32x4 of0 = lds128(L, a_of), of1 = lds128(L, a_of + 64);
      f32x4 a = {0.f, 0.f, 0.f, 0.f};
      a = mfma(of0, wof[0], a);
      a = mfma(of1, wof[1], a);
      if (lane < 16) stmb(rs, m_h, u32x4{pk(a.x, a.y), tA, pk(a.z, a.w), tB});
    }
    BAR();
  }
}

extern "C" void kernel_launch(void* const* d_in, const int* in_sizes, int n_in, void* d_out, int out_size, void* d_ws, size_t ws_size,
                              hipStream_t stream) {
  fused_decode_kernel<<<256, 768, 0, stream>>>((const u16*)d_in[0], (const u16*)d_in[1], (const u16*)d_in[2], (const u16*)d_in[3],
                                               (const u16*)d_in[4], (const u16*)d_in[6], (const u16*)d_in[7], (const u16*)d_in[8],
                                               (u16*)d_out, d_ws);
}
```
